```python
import torch

def _softmax(x, axis=-1):
    return torch.softmax(x, dim=axis)
def _gelu(x):
    return torch.nn.functional.gelu(x, approximate="tanh")
def _silu(x):
    return torch.nn.functional.silu(x)
def _sigmoid(x):
    return torch.sigmoid(x)
def _layernorm(x, axis=-1, eps=1e-5):
    m = x.mean(dim=axis, keepdim=True)
    v = x.var(dim=axis, keepdim=True, unbiased=False)
    return (x - m) / torch.sqrt(v + eps)
def _scatter_add(tgt, idx, src, axis=0):
    return tgt.clone().index_add_(axis, idx, src)
def _segment_sum(src, seg, n):
    out = torch.zeros((n,) + src.shape[1:], dtype=src.dtype)
    return out.index_add_(0, seg, src)
def _pad(x, pw):
    flat = tuple(v for pair in reversed(pw) for v in pair)
    return torch.nn.functional.pad(x, flat)

kernel_name = 'gated_mix__linear_kernel_hedgehog__glu_gated_geglu__prompt'

def setup_inputs():

    _w = (lambda a: (lambda t: t.to(torch.bfloat16) if t.is_floating_point() else t)(torch.as_tensor(a)))
    def _A_setup():
        B, S, D, H = 1, 2048, 1024, 32
        Dh = D // H
        return {
            "x": _w((lambda *s: torch.randn(*s, dtype=torch.bfloat16) * 0.05)(B, S, D)),
            "wqkv": _w((lambda *s: torch.randn(*s, dtype=torch.bfloat16) * 0.8)(D, 3 * D)),
            "w_phi": _w((lambda *s: torch.randn(*s, dtype=torch.bfloat16) * 0.8)(H, Dh, Dh) * (Dh ** -0.5)),
            "wo": _w((lambda *s: torch.randn(*s, dtype=torch.bfloat16) * 0.05)(D, D)),
        }
    _A_in = _A_setup()

    def _B_setup():
        B, S, Dm, Df = 1, 2048, 1024, 3584
        return {
            "x": _w((lambda *s: torch.randn(*s, dtype=torch.bfloat16) * 0.05)(B, S, Dm)),
            "w_gate": _w((lambda *s: torch.randn(*s, dtype=torch.bfloat16) * 1.6)(Dm, Df) * (Dm ** -0.5)),
            "w_up": _w((lambda *s: torch.randn(*s, dtype=torch.bfloat16) * 1.6)(Dm, Df) * (Dm ** -0.5)),
            "w_down": _w((lambda *s: torch.randn(*s, dtype=torch.bfloat16) * 1.6)(Df, Dm) * (Df ** -0.5)),
        }
    _B_in = _B_setup()

    return {
        "A_x": _A_in["x"],
        "A_wqkv": _A_in["wqkv"],
        "A_w_phi": _A_in["w_phi"],
        "A_wo": _A_in["wo"],
        "B_x": _B_in["x"],
        "B_w_gate": _B_in["w_gate"],
        "B_w_up": _B_in["w_up"],
        "B_w_down": _B_in["w_down"],
        "gate_w": _w((lambda *s: torch.randn(*s, dtype=torch.bfloat16) * 0.05)(1024) * 16.0),
    }

def reference(A_x, A_wqkv, A_w_phi, A_wo, B_x, B_w_gate, B_w_up, B_w_down, gate_w):
    def _A_ref(x, wqkv, w_phi, wo):
        B, S, D = x.shape
        H = 32; Dh = D // H
        qkv = (lambda x, *d: x.permute(*d))((x @ wqkv).reshape(B, S, 3, H, Dh), 2, 0, 3, 1, 4)
        q, k, v = qkv[0], qkv[1], qkv[2]
        pq = torch.einsum("bhsd,hdf->bhsf", q, w_phi)
        pk = torch.einsum("bhsd,hdf->bhsf", k, w_phi)
        phi_q = torch.concatenate([_softmax(pq, axis=-1), _softmax(-pq, axis=-1)], axis=-1)
        phi_k = torch.concatenate([_softmax(pk, axis=-1), _softmax(-pk, axis=-1)], axis=-1)
        kv = torch.einsum("bhsf,bhsd->bhfd", phi_k, v)
        z  = torch.einsum("bhsf->bhf", phi_k)
        num = torch.einsum("bhqf,bhfd->bhqd", phi_q, kv)
        den = torch.einsum("bhqf,bhf->bhq", phi_q, z)[..., None] + 1e-6
        o = num / den
        return (lambda x, *d: x.permute(*d))(o, 0, 2, 1, 3).reshape(B, S, D) @ wo

    def _B_ref(x, w_gate, w_up, w_down):
        g = torch.einsum("bsd,df->bsf", x, w_gate)
        u = torch.einsum("bsd,df->bsf", x, w_up)
        h = _gelu(g) * u
        return torch.einsum("bsf,fd->bsd", h, w_down)

    x = A_x
    g = _sigmoid((x * gate_w).sum(-1, keepdims=True))
    a = _A_ref(x, A_wqkv, A_w_phi, A_wo)
    b = _B_ref(x, B_w_gate, B_w_up, B_w_down)
    return g * a + (1 - g) * b
```

```cpp
#include <hip/hip_runtime.h>
#include <hip/hip_bf16.h>
#include <stdint.h>
#include <type_traits>
#pragma clang diagnostic ignored "-Wint-to-pointer-cast"

typedef short bf16x8 __attribute__((ext_vector_type(8)));
typedef short s16x4 __attribute__((ext_vector_type(4)));
typedef float f32x4 __attribute__((ext_vector_type(4)));
typedef float f32x2 __attribute__((ext_vector_type(2)));
typedef unsigned u32x4 __attribute__((ext_vector_type(4)));
typedef unsigned u32x2 __attribute__((ext_vector_type(2)));
typedef __bf16 bf16x2_t __attribute__((ext_vector_type(2)));
#define LAS __attribute__((address_space(3)))
#define GAS __attribute__((address_space(1)))
#define DI __device__ __forceinline__

constexpr int SEQ = 2048, DM = 1024, DF = 3584;
constexpr size_t WS_FLAGS = 0;
constexpr size_t WS_G     = 32768;
constexpr size_t WS_ZP    = 65536;
constexpr size_t WS_KVP   = 262144;
constexpr size_t WS_PHIQ  = 4u << 20;
constexpr size_t WS_AP    = 16u << 20;
constexpr size_t WS_PART  = 40u << 20;

DI f32x4 mfma16(bf16x8 a, bf16x8 b, f32x4 c) { return __builtin_amdgcn_mfma_f32_16x16x32_bf16(a, b, c, 0, 0, 0); }
DI s16x4 trrd(unsigned a) { return __builtin_amdgcn_ds_read_tr16_b64_v4i16((LAS s16x4*)a); }
DI bf16x8 trrd2(unsigned a0, unsigned a1) { s16x4 lo = trrd(a0), hi = trrd(a1); return __builtin_shufflevector(lo, hi, 0, 1, 2, 3, 4, 5, 6, 7); }
DI bf16x8 ldsrd(unsigned a) { return *(LAS bf16x8*)a; }
DI unsigned pk2(float lo, float hi) { f32x2 v = {lo, hi}; bf16x2_t b = __builtin_convertvector(v, bf16x2_t); return __builtin_bit_cast(unsigned, b); }
DI bf16x8 pack8(f32x4 a, f32x4 b) { u32x4 r = {pk2(a[0], a[1]), pk2(a[2], a[3]), pk2(b[0], b[1]), pk2(b[2], b[3])}; return __builtin_bit_cast(bf16x8, r); }
DI float bfr(float x) { unsigned u = pk2(x, x); return __uint_as_float(u << 16); }
DI u32x4 mk_srd(const void* p, unsigned bytes) {
  uint64_t a = (uint64_t)p;
  u32x4 r;
  r[0] = __builtin_amdgcn_readfirstlane((unsigned)a);
  r[1] = __builtin_amdgcn_readfirstlane((unsigned)(a >> 32) & 0xffffu);
  r[2] = __builtin_amdgcn_readfirstlane(bytes);
  r[3] = 0x00020000u;
  return r;
}
DI void dma16(u32x4 srd, unsigned lds, unsigned voff, unsigned soff) {
  asm volatile("s_mov_b32 m0, %0\n\ts_nop 0\n\tbuffer_load_dwordx4 %1, %2, %3 offen lds" ::"s"(lds), "v"(voff), "s"(srd), "s"(soff) : "memory");
}
#define WAIT_BAR(N) asm volatile("s_waitcnt vmcnt(" #N ") lgkmcnt(0)\n\ts_barrier" ::: "memory")
#define SB() __builtin_amdgcn_sched_barrier(0)

DI float xmax16(float v) { auto r = __builtin_amdgcn_permlane16_swap(__float_as_uint(v), __float_as_uint(v), false, false); return fmaxf(__uint_as_float(r[0]), __uint_as_float(r[1])); }
DI float xmax32(float v) { auto r = __builtin_amdgcn_permlane32_swap(__float_as_uint(v), __float_as_uint(v), false, false); return fmaxf(__uint_as_float(r[0]), __uint_as_float(r[1])); }
DI float xmin16(float v) { auto r = __builtin_amdgcn_permlane16_swap(__float_as_uint(v), __float_as_uint(v), false, false); return fminf(__uint_as_float(r[0]), __uint_as_float(r[1])); }
DI float xmin32(float v) { auto r = __builtin_amdgcn_permlane32_swap(__float_as_uint(v), __float_as_uint(v), false, false); return fminf(__uint_as_float(r[0]), __uint_as_float(r[1])); }
DI float xadd16(float v) { auto r = __builtin_amdgcn_permlane16_swap(__float_as_uint(v), __float_as_uint(v), false, false); return __uint_as_float(r[0]) + __uint_as_float(r[1]); }
DI float xadd32(float v) { auto r = __builtin_amdgcn_permlane32_swap(__float_as_uint(v), __float_as_uint(v), false, false); return __uint_as_float(r[0]) + __uint_as_float(r[1]); }

DI void featmap(f32x4 a0, f32x4 a1, bf16x8 wp0, bf16x8 wp1, f32x4& pp0, f32x4& pp1, f32x4& pn0, f32x4& pn1) {
  const bf16x8 fr = pack8(a0, a1);
  const f32x4 z4 = {0.f, 0.f, 0.f, 0.f};
  f32x4 p0 = mfma16(wp0, fr, z4), p1 = mfma16(wp1, fr, z4);
#pragma unroll
  for (int r = 0; r < 4; ++r) { p0[r] = bfr(p0[r]); p1[r] = bfr(p1[r]); }
  float mx = fmaxf(fmaxf(fmaxf(p0[0], p0[1]), fmaxf(p0[2], p0[3])), fmaxf(fmaxf(p1[0], p1[1]), fmaxf(p1[2], p1[3])));
  float mn = fminf(fminf(fminf(p0[0], p0[1]), fminf(p0[2], p0[3])), fminf(fminf(p1[0], p1[1]), fminf(p1[2], p1[3])));
  mx = xmax32(xmax16(mx));
  mn = xmin32(xmin16(mn));
  constexpr float L2E = 1.4426950408889634f;
  const float mxl = mx * L2E, mnl = mn * L2E;
  float sp = 0.f, sn = 0.f;
#pragma unroll
  for (int r = 0; r < 4; ++r) {
    pp0[r] = __builtin_amdgcn_exp2f(p0[r] * L2E - mxl); pp1[r] = __builtin_amdgcn_exp2f(p1[r] * L2E - mxl);
    pn0[r] = __builtin_amdgcn_exp2f(mnl - p0[r] * L2E); pn1[r] = __builtin_amdgcn_exp2f(mnl - p1[r] * L2E);
    sp += pp0[r] + pp1[r]; sn += pn0[r] + pn1[r];
  }
  sp = xadd32(xadd16(sp));
  sn = xadd32(xadd16(sn));
  const float ip = __builtin_amdgcn_rcpf(sp), in = __builtin_amdgcn_rcpf(sn);
  pp0 *= ip; pp1 *= ip; pn0 *= in; pn1 *= in;
}

DI f32x4 geglu4(f32x4 g, f32x4 u) {
  constexpr float C1 = -2.0f * 0.7978845608028654f * 1.4426950408889634f, C2 = C1 * 0.044715f;
  f32x4 o;
#pragma unroll
  for (int r = 0; r < 4; ++r) {
    const float gg = g[r];
    const float arg = gg * (C1 + C2 * gg * gg);
    const float e = __builtin_amdgcn_exp2f(arg);
    o[r] = gg * u[r] * __builtin_amdgcn_rcpf(1.0f + e);
  }
  return o;
}

constexpr unsigned K1_X0 = 0, K1_XS = 32768, K1_W0 = 65536, K1_WS = 40960, K1_WG = 12288, K1_WU = 26624, K1_PHI = 147456, K1_LDS = 149504;

template <int WN>
DI void k1_body(f32x4 (&acc)[4][10], const unsigned lb, const int wm, const int lane,
                const u32x4 sx, const unsigned vxa, const unsigned vxb, unsigned sxo, const unsigned xl,
                const u32x4 (&sw)[5], const unsigned (&vw)[5], const unsigned (&wst)[5], const unsigned wl) {
  const int g = lane >> 4, q = (lane & 15) >> 2, p = lane & 3, fr = lane & 15;
  const unsigned xrow = wm * 64 + fr;
  const unsigned xb0 = lb + K1_X0 + xrow * 128 + ((g ^ ((xrow >> 1) & 7)) << 4);
  const unsigned xb1 = xb0 ^ 64;
  const unsigned qb0 = lb + K1_W0 + (8 * g + q) * 192 + ((0 ^ (g & 1)) << 5) + p * 8;
  const unsigned qb1 = lb + K1_W0 + (8 * g + q) * 192 + ((1 ^ (g & 1)) << 5) + p * 8;
  const unsigned gb0 = lb + K1_W0 + K1_WG + (8 * g + 4 * (0 ^ (g & 1)) + q) * 224 + p * 8;
  const unsigned gb1 = lb + K1_W0 + K1_WG + (8 * g + 4 * (1 ^ (g & 1)) + q) * 224 + p * 8;
  unsigned swo[5];
#pragma unroll
  for (int i = 0; i < 5; ++i) swo[i] = 2 * wst[i];
  sxo += 128;

  bf16x8 xf[2][4], wf[4];
  auto ldW = [&](int st, int u) -> bf16x8 {
    const int ks = u / 10, i = u % 10;
    const unsigned so = st * K1_WS;
    constexpr int NQ = WN == 0 ? 4 : 2;
    constexpr int NG = WN == 0 ? 3 : 4;
    constexpr int P0 = WN == 0 ? 4 : 0;
    if (i < NQ) {
      const int pt = WN == 0 ? 1 + (i >> 1) : 0, nh = i & 1;
      const unsigned b = (nh ? qb1 : qb0) + so + ks * 32 * 192 + pt * 64;
      return trrd2(b, b + 4 * 192);
    } else {
      const int j = i - NQ, up = j / NG, P = P0 + j % NG;
      const unsigned o = so + ks * 32 * 224 + up * (K1_WU - K1_WG) + P * 32;
      return trrd2(gb0 + o, gb1 + o);
    }
  };
  auto ldX = [&](int st, int ks, int mt) -> bf16x8 { return ldsrd((ks ? xb1 : xb0) + st * K1_XS + mt * 2048); };
  auto mma = [&](int u) {
    const int ks = u / 10, i = u % 10;
#pragma unroll
    for (int mt = 0; mt < 4; ++mt) {
      if (WN == 0 && (i == 2 || i == 3)) acc[mt][i] = mfma16(xf[ks][mt], wf[u & 3], acc[mt][i]);
      else acc[mt][i] = mfma16(wf[u & 3], xf[ks][mt], acc[mt][i]);
    }
  };
  auto dma = [&](int st, int j) {
    if (j < 4) dma16(sx, xl + st * K1_XS + j * 1024, (j & 1) ? vxb : vxa, sxo + j * 16384);
    else dma16(sw[j - 4], wl + st * K1_WS + (j - 4) * 1024, vw[j - 4], swo[j - 4]);
  };

#pragma unroll
  for (int mt = 0; mt < 4; ++mt) xf[0][mt] = ldX(0, 0, mt);
  wf[0] = ldW(0, 0); wf[1] = ldW(0, 1); wf[2] = ldW(0, 2);

  auto step = [&](auto st_, auto dox_, auto dow_) {
    constexpr int st = decltype(st_)::value;
    constexpr bool dox = decltype(dox_)::value, dow = decltype(dow_)::value;
#pragma unroll
    for (int u = 0; u < 17; ++u) {
      wf[(u + 3) & 3] = ldW(st, u + 3);
      if (u >= 2 && u < 6) xf[1][u - 2] = ldX(st, 1, u - 2);
      if (u < 4 && dox) dma(st ^ 1, u);
      mma(u);
      SB();
    }
    if (dox) sxo += 128;
    mma(17);
    SB();
    WAIT_BAR(0);
    if (dox) {
#pragma unroll
      for (int mt = 0; mt < 4; ++mt) xf[0][mt] = ldX(st ^ 1, 0, mt);
      wf[0] = ldW(st ^ 1, 0); wf[1] = ldW(st ^ 1, 1);
    }
    if (dow) { dma(st, 4); dma(st, 5); dma(st, 6); }
    mma(18);
    SB();
    if (dox) wf[2] = ldW(st ^ 1, 2);
    if (dow) {
      dma(st, 7); dma(st, 8);
#pragma unroll
      for (int i = 0; i < 5; ++i) swo[i] += wst[i];
    }
    mma(19);
    SB();
  };
  using I0 = std::integral_constant<int, 0>; using I1 = std::integral_constant<int, 1>;
  using BT = std::true_type; using BF = std::false_type;
  for (int t2 = 0; t2 < 7; ++t2) { step(I0{}, BT{}, BT{}); step(I1{}, BT{}, BT{}); }
  step(I0{}, BT{}, BF{});
  step(I1{}, BF{}, BF{});
}

__global__ __launch_bounds__(512, 2) void k1_kernel(const uint16_t* __restrict__ x, const uint16_t* __restrict__ wqkv, const uint16_t* __restrict__ wphi,
                                                    const uint16_t* __restrict__ wg, const uint16_t* __restrict__ wu, const uint16_t* __restrict__ gatew,
                                                    char* __restrict__ ws) {
  extern __shared__ __attribute__((aligned(16))) char smem[];
  const unsigned lb = (unsigned)(size_t)(LAS char*)smem;
  const int tid = threadIdx.x, lane = tid & 63;
  const int w = __builtin_amdgcn_readfirstlane(tid >> 6);
  const int wn = w >> 2, wm = w & 3;
  const int b = blockIdx.x, xj = b & 7, bi = b >> 3;
  const int c = 4 * xj + (bi & 3), rb = bi >> 2;

  const u32x4 sx = mk_srd(x, SEQ * DM * 2);
  const unsigned xr = 32 * w + (lane >> 3);
  const unsigned vxa = (rb * 256 + xr) * 2048 + (((lane & 7) ^ (lane >> 4)) << 4);
  const unsigned vxb = vxa ^ 64;
  const unsigned xl = lb + K1_X0 + w * 4096;
  u32x4 sw[5]; unsigned vw[5], wst[5];
#pragma unroll
  for (int i = 0; i < 5; ++i) {
    const int iw = 5 * w + i;
    if (iw < 12) {
      const int ci = 64 * iw + lane, k = ci / 12, cc = ci % 12;
      const int pt = cc >> 2, nh = ((cc >> 1) & 1) ^ ((k >> 3) & 1), hf = cc & 1;
      vw[i] = k * 6144 + (pt * 1024 + 32 * c + nh * 16 + hf * 8) * 2;
    } else {
      const int ci = 64 * (iw < 26 ? iw - 12 : iw - 26) + lane, rho = ci / 14, cc = ci % 14;
      const int k = rho ^ (((rho >> 3) & 1) << 2);
      const int P = cc >> 1, G = (c & 1) ? (P < 4 ? P + 3 : (P < 6 ? P - 3 : 0)) : P;
      vw[i] = k * 7168 + (112 * c + 16 * G + 8 * (cc & 1)) * 2;
    }
    const uint16_t* base = iw < 12 ? wqkv : (iw < 26 ? wg : wu);
    sw[i] = mk_srd(base, iw < 12 ? DM * 3072 * 2 : DM * DF * 2);
    wst[i] = __builtin_amdgcn_readfirstlane(iw < 12 ? 64 * 6144 : 64 * 7168);
  }
  const unsigned wl = lb + K1_W0 + w * 5120;
  const int grow = b * 8 + w;
  const u32x4 gx0 = *(const u32x4*)(x + grow * DM + lane * 8), gx1 = *(const u32x4*)(x + grow * DM + 512 + lane * 8);
  const u32x4 gw0 = *(const u32x4*)(gatew + lane * 8), gw1 = *(const u32x4*)(gatew + 512 + lane * 8);
  SB();
#pragma unroll
  for (int i = 0; i < 5; ++i) dma16(sw[i], wl + i * 1024, vw[i], 0u);
#pragma unroll
  for (int j = 0; j < 4; ++j) dma16(sx, xl + j * 1024, (j & 1) ? vxb : vxa, j * 16384);
#pragma unroll
  for (int i = 0; i < 5; ++i) dma16(sw[i], wl + K1_WS + i * 1024, vw[i], wst[i]);
  if (w < 2) {
    const u32x4 sp = mk_srd(wphi, 32 * 32 * 32 * 2);
    dma16(sp, lb + K1_PHI + w * 1024, lane * 16, c * 2048 + w * 1024);
  }
  SB();
  {
    float s = 0.f;
#pragma unroll
    for (int i = 0; i < 4; ++i) {
      s += bfr(__uint_as_float(gx0[i] << 16) * __uint_as_float(gw0[i] << 16)) + bfr(__uint_as_float(gx0[i] & 0xffff0000u) * __uint_as_float(gw0[i] & 0xffff0000u));
      s += bfr(__uint_as_float(gx1[i] << 16) * __uint_as_float(gw1[i] << 16)) + bfr(__uint_as_float(gx1[i] & 0xffff0000u) * __uint_as_float(gw1[i] & 0xffff0000u));
    }
#pragma unroll
    for (int o = 1; o < 64; o <<= 1) s += __shfl_xor(s, o);
    s = bfr(s);
    const float gv = bfr(1.0f / (1.0f + __expf(-s)));
    if (lane == 0) ((float*)(ws + WS_G))[grow] = gv;
    if (tid == 0) {
      if (b < 8) ((unsigned*)(ws + WS_FLAGS))[b * 32] = 0u;
      if (b < 128) ((unsigned*)(ws + WS_FLAGS))[(8 + b) * 32] = 0u;
    }
  }
  f32x4 acc[4][10];
#pragma unroll
  for (int m = 0; m < 4; ++m)
#pragma unroll
    for (int i = 0; i < 10; ++i) acc[m][i] = (f32x4){0.f, 0.f, 0.f, 0.f};
  WAIT_BAR(0);

  const int m16 = rb * 16 + wm * 4;
  char* const ap = ws + WS_AP;
#define EPI_LANE() int lane = __builtin_amdgcn_mbcnt_hi(~0u, __builtin_amdgcn_mbcnt_lo(~0u, 0u)); asm volatile("" : "+v"(lane)); \
  const int g = lane >> 4, q = (lane & 15) >> 2, p = lane & 3; const unsigned wpb = lb + K1_PHI + (4 * g + q) * 64 + p * 8;
  if (wn == 0) {
    k1_body<0>(acc, lb, wm, lane, sx, vxa, vxb, 0u, xl, sw, vw, wst, wl);
    EPI_LANE();
    const bf16x8 wp0 = trrd2(wpb, wpb + 16 * 64), wp1 = trrd2(wpb + 32, wpb + 16 * 64 + 32);
    const unsigned pkb = lb + wm * 8704;
    const unsigned pw = pkb + (lane & 15) * 136 + g * 8;
#pragma unroll
    for (int mt = 0; mt < 4; ++mt) {
      f32x4 pp0, pp1, pn0, pn1;
      featmap(acc[mt][0], acc[mt][1], wp0, wp1, pp0, pp1, pn0, pn1);
      *(LAS u32x2*)(pw + mt * 16 * 136 + 0)  = (u32x2){pk2(pp0[0], pp0[1]), pk2(pp0[2], pp0[3])};
      *(LAS u32x2*)(pw + mt * 16 * 136 + 32) = (u32x2){pk2(pp1[0], pp1[1]), pk2(pp1[2], pp1[3])};
      *(LAS u32x2*)(pw + mt * 16 * 136 + 64) = (u32x2){pk2(pn0[0], pn0[1]), pk2(pn0[2], pn0[3])};
      *(LAS u32x2*)(pw + mt * 16 * 136 + 96) = (u32x2){pk2(pn1[0], pn1[1]), pk2(pn1[2], pn1[3])};
    }
    asm volatile("s_waitcnt lgkmcnt(0)" ::: "memory");
    f32x4 kv[4][2], zz[4];
#pragma unroll
    for (int ft = 0; ft < 4; ++ft) { kv[ft][0] = (f32x4){0.f, 0.f, 0.f, 0.f}; kv[ft][1] = kv[ft][0]; zz[ft] = kv[ft][0]; }
    const bf16x8 ones = {0x3f80, 0x3f80, 0x3f80, 0x3f80, 0x3f80, 0x3f80, 0x3f80, 0x3f80};
    const unsigned pr = pkb + (4 * g + q) * 136 + p * 8;
#pragma unroll
    for (int mp = 0; mp < 2; ++mp) {
      const bf16x8 v0 = pack8(acc[2 * mp][2], acc[2 * mp + 1][2]), v1 = pack8(acc[2 * mp][3], acc[2 * mp + 1][3]);
#pragma unroll
      for (int ft = 0; ft < 4; ++ft) {
        const bf16x8 a = trrd2(pr + (32 * mp) * 136 + ft * 32, pr + (32 * mp + 16) * 136 + ft * 32);
        kv[ft][0] = mfma16(a, v0, kv[ft][0]);
        kv[ft][1] = mfma16(a, v1, kv[ft][1]);
        zz[ft] = mfma16(a, ones, zz[ft]);
      }
    }
    const unsigned rd = lb + 65536 + wm * 8192 + lane * 16;
#pragma unroll
    for (int ft = 0; ft < 4; ++ft) {
      *(LAS f32x4*)(rd + (ft * 2 + 0) * 1024) = kv[ft][0];
      *(LAS f32x4*)(rd + (ft * 2 + 1) * 1024) = kv[ft][1];
      if ((lane & 15) == 0) *(LAS f32x4*)(lb + 65536 + 32768 + wm * 256 + ft * 64 + g * 16) = zz[ft];
    }
    const int jp = 7 * c + ((c & 1) ? 1 : 4), js = 7 * c + ((c & 1) ? 0 : 6);
    char* const hp = ap + ((size_t)(jp >> 1) * 128 + m16) * 1024 + lane * 16;
    char* const hs = ap + ((size_t)(js >> 1) * 128 + m16) * 1024 + lane * 16 + (js & 1) * 8;
#pragma unroll
    for (int mt = 0; mt < 4; ++mt) {
      const f32x4 h0 = geglu4(acc[mt][4], acc[mt][7]), h1 = geglu4(acc[mt][5], acc[mt][8]), h2 = geglu4(acc[mt][6], acc[mt][9]);
      *(bf16x8*)(hp + mt * 1024) = pack8(h0, h1);
      *(u32x2*)(hs + mt * 1024) = (u32x2){pk2(h2[0], h2[1]), pk2(h2[2], h2[3])};
    }
  } else {
    k1_body<1>(acc, lb, wm, lane, sx, vxa, vxb, 0u, xl, sw, vw, wst, wl);
    EPI_LANE();
    const bf16x8 wp0 = trrd2(wpb, wpb + 16 * 64), wp1 = trrd2(wpb + 32, wpb + 16 * 64 + 32);
    char* const pq = ws + WS_PHIQ + ((size_t)c * 128 + m16) * 2048 + lane * 16;
#pragma unroll
    for (int mt = 0; mt < 4; ++mt) {
      f32x4 pp0, pp1, pn0, pn1;
      featmap(acc[mt][0], acc[mt][1], wp0, wp1, pp0, pp1, pn0, pn1);
      *(bf16x8*)(pq + mt * 2048) = pack8(pp0, pp1);
      *(bf16x8*)(pq + mt * 2048 + 1024) = pack8(pn0, pn1);
    }
    const int j0 = 7 * c + ((c & 1) ? 3 : 0);
    char* const hp = ap + ((size_t)(j0 >> 1) * 128 + m16) * 1024 + lane * 16;
#pragma unroll
    for (int mt = 0; mt < 4; ++mt) {
      const f32x4 h0 = geglu4(acc[mt][2], acc[mt][6]), h1 = geglu4(acc[mt][3], acc[mt][7]);
      *(bf16x8*)(hp + mt * 1024) = pack8(h0, h1);
      const f32x4 h2 = geglu4(acc[mt][4], acc[mt][8]), h3 = geglu4(acc[mt][5], acc[mt][9]);
      *(bf16x8*)(hp + 131072 + mt * 1024) = pack8(h2, h3);
    }
  }
  asm volatile("s_waitcnt lgkmcnt(0)\n\ts_barrier" ::: "memory");
  {
    int tid = w * 64 + __builtin_amdgcn_mbcnt_hi(~0u, __builtin_amdgcn_mbcnt_lo(~0u, 0u)); asm volatile("" : "+v"(tid));
    const unsigned rd = lb + 65536 + tid * 16;
    const f32x4 s = *(LAS f32x4*)(rd) + *(LAS f32x4*)(rd + 8192) + *(LAS f32x4*)(rd + 16384) + *(LAS f32x4*)(rd + 24576);
    *(f32x4*)(ws + WS_KVP + ((size_t)(c * 8 + rb) * 512 + tid) * 16) = s;
    if (tid < 16) {
      const unsigned rz = lb + 65536 + 32768 + tid * 16;
      const f32x4 zs = *(LAS f32x4*)(rz) + *(LAS f32x4*)(rz + 256) + *(LAS f32x4*)(rz + 512) + *(LAS f32x4*)(rz + 768);
      *(f32x4*)(ws + WS_ZP + ((size_t)(c * 8 + rb) * 16 + tid) * 16) = zs;
    }
  }
}

constexpr unsigned K2_SLOT = 32768, K2_B = 16384, K2_KV = 131072, K2_LDS = 131072 + 4096 + 256;
constexpr int T_OWN = 39, T_ALL = 72, T_O = 56;

DI bool wait_ge(const unsigned* flag, unsigned need) {
  for (unsigned spin = 0; spin < (1u << 22); ++spin) {
    if (__hip_atomic_load((const GAS unsigned*)flag, __ATOMIC_RELAXED, __HIP_MEMORY_SCOPE_AGENT) >= need) return true;
    __builtin_amdgcn_s_sleep(2);
  }
  return false;
}

__global__ __launch_bounds__(512, 2) void k2_kernel(const uint16_t* __restrict__ wdown, const uint16_t* __restrict__ wo, char* __restrict__ ws,
                                                    uint16_t* __restrict__ out) {
  extern __shared__ __attribute__((aligned(16))) char smem[];
  const unsigned lb = (unsigned)(size_t)(LAS char*)smem;
  const int tid = threadIdx.x, lane = tid & 63;
  const int w = __builtin_amdgcn_readfirstlane(tid >> 6);
  const int g = lane >> 4, q = (lane & 15) >> 2, p = lane & 3;
  const int b = blockIdx.x, xj = b & 7, bi = b >> 3;
  const int helper = xj >> 2;
  const int mt128 = 4 * (xj & 3) + (bi >> 3), nt128 = bi & 7;
  const int pair = mt128 * 8 + nt128;
  const int kg = w >> 2, wm = (w >> 1) & 1, wn = w & 1;
  const int T0 = helper ? T_OWN : 0, T1 = helper ? T_ALL : T_OWN;
  unsigned* const flags = (unsigned*)(ws + WS_FLAGS);
  const float* const gs = (const float*)(ws + WS_G);

  const int oc = 4 * xj + (bi & 3), orb = bi >> 2;
  f32x4 kvs;
  {
    const char* kp = ws + WS_KVP + ((size_t)oc * 8 * 512 + tid) * 16;
    kvs = *(const f32x4*)kp;
#pragma unroll
    for (int r = 1; r < 8; ++r) kvs += *(const f32x4*)(kp + r * 8192);
  }
  f32x4 zs = {0.f, 0.f, 0.f, 0.f};
  if (tid < 16) {
    const char* zp = ws + WS_ZP + ((size_t)oc * 8 * 16 + tid) * 16;
#pragma unroll
    for (int r = 0; r < 8; ++r) zs += *(const f32x4*)(zp + r * 256);
  }
  bf16x8 pf[2][2];
  float go[2];
  {
    const char* pq = ws + WS_PHIQ + ((size_t)oc * 128 + orb * 16 + 2 * w) * 2048 + lane * 16;
#pragma unroll
    for (int i = 0; i < 2; ++i) { pf[i][0] = *(const bf16x8*)(pq + i * 2048); pf[i][1] = *(const bf16x8*)(pq + i * 2048 + 1024); go[i] = gs[(orb * 16 + 2 * w + i) * 16 + (lane & 15)]; }
  }
  float gm[4];
#pragma unroll
  for (int mt = 0; mt < 4; ++mt) gm[mt] = gs[mt128 * 128 + wm * 64 + mt * 16 + (lane & 15)];
  SB();

  const u32x4 sa = mk_srd(ws + WS_AP, 144u * 131072u);
  const u32x4 sd = mk_srd(wdown, DF * DM * 2), so_ = mk_srd(wo, DM * DM * 2);
  const unsigned va = lane * 16;
  unsigned vb[2];
#pragma unroll
  for (int i = 0; i < 2; ++i) { const int k = 8 * w + 4 * i + g; vb[i] = k * 2048 + nt128 * 256 + (((lane & 15) ^ ((k & 7) << 1)) << 4); }
  const unsigned a_so = (w >> 2) * 131072 + (mt128 * 8 + 2 * (w & 3)) * 1024;
  auto issue = [&](int T) {
    const unsigned sl = lb + (T & 3) * K2_SLOT;
    const unsigned ao = a_so + T * 262144;
    dma16(sa, sl + w * 2048, va, ao);
    dma16(sa, sl + w * 2048 + 1024, va, ao + 1024);
    const bool iso = T >= T_O;
    u32x4 sb_;
#pragma unroll
    for (int i = 0; i < 4; ++i) sb_[i] = iso ? so_[i] : sd[i];
    const unsigned bo = (iso ? T - T_O : T) * 131072;
    dma16(sb_, sl + K2_B + w * 2048, vb[0], bo);
    dma16(sb_, sl + K2_B + w * 2048 + 1024, vb[1], bo);
  };
  issue(T0); issue(T0 + 1); issue(T0 + 2);
  SB();

  {
    const int tile = tid >> 6, ft = tile >> 1, dt = tile & 1;
    *(LAS u32x2*)(lb + K2_KV + (dt * 2 + (ft >> 1)) * 1024 + lane * 16 + (ft & 1) * 8) = (u32x2){pk2(kvs[0], kvs[1]), pk2(kvs[2], kvs[3])};
    if (tid < 16) {
      *(LAS u32x2*)(lb + K2_KV + 4096 + (tid >> 3) * 64 + (tid & 3) * 16 + ((tid >> 2) & 1) * 8) = (u32x2){pk2(zs[0], zs[1]), pk2(zs[2], zs[3])};
    }
    asm volatile("s_waitcnt lgkmcnt(0)\n\ts_barrier" ::: "memory");
    bf16x8 ka[2][2], za[2];
#pragma unroll
    for (int ks = 0; ks < 2; ++ks) {
      ka[0][ks] = ldsrd(lb + K2_KV + (0 + ks) * 1024 + lane * 16);
      ka[1][ks] = ldsrd(lb + K2_KV + (2 + ks) * 1024 + lane * 16);
      za[ks] = ldsrd(lb + K2_KV + 4096 + ks * 64 + g * 16);
    }
    const auto rs = __builtin_amdgcn_make_buffer_rsrc((void*)(ws + WS_AP), 0, 144u * 131072u, 0x00020000);
#pragma unroll
    for (int i = 0; i < 2; ++i) {
      const f32x4 z4 = {0.f, 0.f, 0.f, 0.f};
      f32x4 n0 = mfma16(ka[0][1], pf[i][1], mfma16(ka[0][0], pf[i][0], z4));
      f32x4 n1 = mfma16(ka[1][1], pf[i][1], mfma16(ka[1][0], pf[i][0], z4));
      f32x4 dn = mfma16(za[1], pf[i][1], mfma16(za[0], pf[i][0], z4));
      const float sc = go[i] / (dn[0] + 1e-6f);
      n0 *= sc; n1 *= sc;
      const bf16x8 o8 = pack8(n0, n1);
      __builtin_amdgcn_raw_buffer_store_b128(__builtin_bit_cast(u32x4, o8), rs, lane * 16, ((112 + oc) * 128 + orb * 16 + 2 * w + i) * 1024, 16  );
    }
    WAIT_BAR(0);
    if (tid == 0) __hip_atomic_fetch_add((GAS unsigned*)(flags + orb * 32), 1u, __ATOMIC_RELAXED, __HIP_MEMORY_SCOPE_AGENT);
  }

  f32x4 acc[4][4];
#pragma unroll
  for (int m = 0; m < 4; ++m)
#pragma unroll
    for (int n = 0; n < 4; ++n) acc[m][n] = (f32x4){0.f, 0.f, 0.f, 0.f};
  const unsigned ab = lb + (kg * 8 + wm * 4) * 1024 + lane * 16;
  unsigned bb[4];
#pragma unroll
  for (int n = 0; n < 4; ++n) {
    const int nt = wn * 4 + n;
    bb[n] = lb + K2_B + (32 * kg + 4 * g + q) * 256 + (((nt ^ (4 * (g & 1) + q)) << 1 | (p >> 1)) << 4) + (p & 1) * 8;
  }
  bf16x8 af[2][4], bf[2][4];
  auto ldF = [&](int set, unsigned sl) {
#pragma unroll
    for (int m = 0; m < 4; ++m) af[set][m] = ldsrd(ab + sl + m * 1024);
#pragma unroll
    for (int n = 0; n < 4; ++n) bf[set][n] = trrd2(bb[n] + sl, bb[n] + sl + 4096);
  };
  auto mma = [&](int set) {
#pragma unroll
    for (int n = 0; n < 4; ++n)
#pragma unroll
      for (int m = 0; m < 4; ++m) acc[m][n] = mfma16(bf[set][n], af[set][m], acc[m][n]);
  };
  ldF(0, (T0 & 3) * K2_SLOT);
  if (T0 + 3 < T1) issue(T0 + 3);
  auto step = [&](int T, int set) {
    if (T + 2 < T1) { WAIT_BAR(8); } else if (T + 1 < T1) { WAIT_BAR(4); } else { WAIT_BAR(0); }
    ldF(set, (T & 3) * K2_SLOT);
    if (T + 3 == T_O && helper) {
      if (w == 0) {
        if (lane == 0) wait_ge(flags + (mt128 >> 1) * 32, 32u);
        __builtin_amdgcn_fence(__ATOMIC_ACQUIRE, "agent");
        asm volatile("s_waitcnt vmcnt(0)" ::: "memory");
      }
      asm volatile("s_barrier" ::: "memory");
    }
    if (T + 3 < T1) issue(T + 3);
    mma(set ^ 1);
    if (T == T_O) {
#pragma unroll
      for (int m = 0; m < 4; ++m) { const float s = bfr(1.0f - gm[m]);
#pragma unroll
        for (int n = 0; n < 4; ++n) acc[m][n] *= s; }
    }
  };
  for (int T = T0 + 1; T < T1; T += 2) { step(T, 1); step(T + 1, 0); }
  mma(0);
  if (!helper) {
#pragma unroll
    for (int m = 0; m < 4; ++m) { const float s = bfr(1.0f - gm[m]);
#pragma unroll
      for (int n = 0; n < 4; ++n) acc[m][n] *= s; }
  }
  asm volatile("s_waitcnt lgkmcnt(0)\n\ts_barrier" ::: "memory");
  {
    const unsigned dst = lb + (w ^ 4) * 8192 + lane * 16;
#pragma unroll
    for (int i = 0; i < 2; ++i)
#pragma unroll
      for (int n = 0; n < 4; ++n) *(LAS f32x4*)(dst + (i * 4 + n) * 1024) = kg ? acc[i][n] : acc[2 + i][n];
  }
  asm volatile("s_waitcnt lgkmcnt(0)\n\ts_barrier" ::: "memory");
  f32x4 fin[2][4];
  {
    const unsigned src = lb + w * 8192 + lane * 16;
#pragma unroll
    for (int i = 0; i < 2; ++i)
#pragma unroll
      for (int n = 0; n < 4; ++n) fin[i][n] = (kg ? acc[2 + i][n] : acc[i][n]) + *(LAS f32x4*)(src + (i * 4 + n) * 1024);
  }
  const auto rp = __builtin_amdgcn_make_buffer_rsrc((void*)(ws + WS_PART), 0, 128u * 65536u, 0x00020000);
  const unsigned po = pair * 65536 + w * 8192;
  if (helper) {
#pragma unroll
    for (int i = 0; i < 2; ++i)
#pragma unroll
      for (int n = 0; n < 4; ++n) __builtin_amdgcn_raw_buffer_store_b128(__builtin_bit_cast(u32x4, fin[i][n]), rp, lane * 16, po + (i * 4 + n) * 1024, 16);
    WAIT_BAR(0);
    if (tid == 0) __hip_atomic_store((GAS unsigned*)(flags + (8 + pair) * 32), 1u, __ATOMIC_RELAXED, __HIP_MEMORY_SCOPE_AGENT);
  } else {
    if (tid == 0) wait_ge(flags + (8 + pair) * 32, 1u);
    asm volatile("s_waitcnt vmcnt(0)\n\ts_barrier" ::: "memory");
#pragma unroll
    for (int i = 0; i < 2; ++i)
#pragma unroll
      for (int n = 0; n < 4; ++n) fin[i][n] += __builtin_bit_cast(f32x4, __builtin_amdgcn_raw_buffer_load_b128(rp, lane * 16, po + (i * 4 + n) * 1024, 16));
#pragma unroll
    for (int i = 0; i < 2; ++i) {
      uint16_t* orow = out + (size_t)(mt128 * 128 + wm * 64 + (2 * kg + i) * 16 + (lane & 15)) * DM + nt128 * 128 + wn * 64 + 4 * g;
#pragma unroll
      for (int n = 0; n < 4; ++n) *(u32x2*)(orow + n * 16) = (u32x2){pk2(fin[i][n][0], fin[i][n][1]), pk2(fin[i][n][2], fin[i][n][3])};
    }
  }
}

extern "C" void kernel_launch(void* const* d_in, const int* in_sizes, int n_in, void* d_out, int out_size, void* d_ws, size_t ws_size,
                              hipStream_t stream) {
  const uint16_t* x = (const uint16_t*)d_in[0];
  const uint16_t* wqkv = (const uint16_t*)d_in[1];
  const uint16_t* wphi = (const uint16_t*)d_in[2];
  const uint16_t* wo = (const uint16_t*)d_in[3];
  const uint16_t* wg = (const uint16_t*)d_in[5];
  const uint16_t* wu = (const uint16_t*)d_in[6];
  const uint16_t* wd = (const uint16_t*)d_in[7];
  const uint16_t* gw = (const uint16_t*)d_in[8];
  (void)hipFuncSetAttribute((const void*)k1_kernel, hipFuncAttributeMaxDynamicSharedMemorySize, K1_LDS);
  (void)hipFuncSetAttribute((const void*)k2_kernel, hipFuncAttributeMaxDynamicSharedMemorySize, K2_LDS);
  k1_kernel<<<256, 512, K1_LDS, stream>>>(x, wqkv, wphi, wg, wu, gw, (char*)d_ws);
  k2_kernel<<<256, 512, K2_LDS, stream>>>(wd, wo, (char*)d_ws, (uint16_t*)d_out);
}
```

```cpp
#include <hip/hip_runtime.h>
#include <hip/hip_bf16.h>
#include <stdint.h>
#include <type_traits>
#pragma clang diagnostic ignored "-Wint-to-pointer-cast"

typedef short bf16x8 __attribute__((ext_vector_type(8)));
typedef short s16x4 __attribute__((ext_vector_type(4)));
typedef float f32x4 __attribute__((ext_vector_type(4)));
typedef float f32x2 __attribute__((ext_vector_type(2)));
typedef unsigned u32x4 __attribute__((ext_vector_type(4)));
typedef unsigned u32x2 __attribute__((ext_vector_type(2)));
typedef __bf16 bf16x2_t __attribute__((ext_vector_type(2)));
#define LAS __attribute__((address_space(3)))
#define GAS __attribute__((address_space(1)))
#define DI __device__ __forceinline__

constexpr int SEQ = 2048, DM = 1024, DF = 3584;
constexpr size_t WS_FLAGS = 0;
constexpr size_t WS_G     = 32768;
constexpr size_t WS_ZP    = 65536;
constexpr size_t WS_KVP   = 262144;
constexpr size_t WS_WPF   = 3u << 20;
constexpr size_t WS_QF    = 4u << 20;
constexpr size_t WS_AP    = 16u << 20;
constexpr size_t WS_PART  = 40u << 20;

#define ABL 0
DI f32x4 mfma16(bf16x8 a, bf16x8 b, f32x4 c) { return __builtin_amdgcn_mfma_f32_16x16x32_bf16(a, b, c, 0, 0, 0); }
DI s16x4 trrd(unsigned a) { return __builtin_amdgcn_ds_read_tr16_b64_v4i16((LAS s16x4*)a); }
DI bf16x8 trrd2(unsigned a0, unsigned a1) { s16x4 lo = trrd(a0), hi = trrd(a1); return __builtin_shufflevector(lo, hi, 0, 1, 2, 3, 4, 5, 6, 7); }
DI bf16x8 ldsrd(unsigned a) { return *(LAS bf16x8*)a; }
DI unsigned pk2(float lo, float hi) { f32x2 v = {lo, hi}; bf16x2_t b = __builtin_convertvector(v, bf16x2_t); return __builtin_bit_cast(unsigned, b); }
DI bf16x8 pack8(f32x4 a, f32x4 b) { u32x4 r = {pk2(a[0], a[1]), pk2(a[2], a[3]), pk2(b[0], b[1]), pk2(b[2], b[3])}; return __builtin_bit_cast(bf16x8, r); }
DI float bfr(float x) { unsigned u = pk2(x, x); return __uint_as_float(u << 16); }
DI u32x4 mk_srd(const void* p, unsigned bytes) {
  uint64_t a = (uint64_t)p;
  u32x4 r;
  r[0] = __builtin_amdgcn_readfirstlane((unsigned)a);
  r[1] = __builtin_amdgcn_readfirstlane((unsigned)(a >> 32) & 0xffffu);
  r[2] = __builtin_amdgcn_readfirstlane(bytes);
  r[3] = 0x00020000u;
  return r;
}
DI void pfetch(u32x4 srd, unsigned lds, unsigned voff, unsigned soff) {
  asm volatile("s_mov_b32 m0, %0\n\ts_nop 0\n\tbuffer_load_dword %1, %2, %3 offen lds" ::"s"(lds), "v"(voff), "s"(srd), "s"(soff) : "memory");
}
DI void dma16(u32x4 srd, unsigned lds, unsigned voff, unsigned soff) {
  asm volatile("s_mov_b32 m0, %0\n\ts_nop 0\n\tbuffer_load_dwordx4 %1, %2, %3 offen lds" ::"s"(lds), "v"(voff), "s"(srd), "s"(soff) : "memory");
}
#define WAIT_BAR(N) do { asm volatile("" ::: "memory"); __builtin_amdgcn_s_waitcnt(((N) & 15) | 0x70 | (((N) >> 4) << 14)); __builtin_amdgcn_s_barrier(); asm volatile("" ::: "memory"); } while (0)
#define LGKM_BAR() do { asm volatile("" ::: "memory"); __builtin_amdgcn_s_waitcnt(0xC07F); __builtin_amdgcn_s_barrier(); asm volatile("" ::: "memory"); } while (0)
#define SB() __builtin_amdgcn_sched_barrier(0)
#define ST(...)
#define WAIT_BAR_T(N) WAIT_BAR(N)

DI float fmx(float a, float b) { return __builtin_amdgcn_fmed3f(a, b, __builtin_inff()); }
#define XRED2(a, b, OP) do { \
    auto r1_ = __builtin_amdgcn_permlane16_swap(__float_as_uint(a), __float_as_uint(b), false, false); \
    float m_ = OP(__uint_as_float(r1_[0]), __uint_as_float(r1_[1])); \
    auto r2_ = __builtin_amdgcn_permlane32_swap(__float_as_uint(m_), __float_as_uint(m_), false, false); \
    m_ = OP(__uint_as_float(r2_[0]), __uint_as_float(r2_[1])); \
    auto r3_ = __builtin_amdgcn_permlane16_swap(__float_as_uint(m_), __float_as_uint(m_), false, false); \
    a = __uint_as_float(r3_[0]); b = __uint_as_float(r3_[1]); } while (0)
DI float fadd_(float a, float b) { return a + b; }
DI f32x4 exp2v(f32x4 v) { return (f32x4){__builtin_amdgcn_exp2f(v[0]), __builtin_amdgcn_exp2f(v[1]), __builtin_amdgcn_exp2f(v[2]), __builtin_amdgcn_exp2f(v[3])}; }

DI void featmap(bf16x8 fr, bf16x8 wp0, bf16x8 wp1, f32x4& pp0, f32x4& pp1, f32x4& pn0, f32x4& pn1) {
  const f32x4 z4 = {0.f, 0.f, 0.f, 0.f};
  constexpr float L2E = 1.4426950408889634f;
  const f32x4 t0 = mfma16(wp0, fr, z4) * L2E, t1 = mfma16(wp1, fr, z4) * L2E;
  float mx = fmaxf(fmaxf(fmaxf(t0[0], t0[1]), fmaxf(t0[2], t0[3])), fmaxf(fmaxf(t1[0], t1[1]), fmaxf(t1[2], t1[3])));
  float nm = -fminf(fminf(fminf(t0[0], t0[1]), fminf(t0[2], t0[3])), fminf(fminf(t1[0], t1[1]), fminf(t1[2], t1[3])));
  XRED2(mx, nm, fmx);
  pp0 = exp2v(t0 - mx); pp1 = exp2v(t1 - mx);
  pn0 = exp2v(-nm - t0); pn1 = exp2v(-nm - t1);
  const f32x4 s4p = pp0 + pp1, s4n = pn0 + pn1;
  float sp = (s4p[0] + s4p[1]) + (s4p[2] + s4p[3]), sn = (s4n[0] + s4n[1]) + (s4n[2] + s4n[3]);
  XRED2(sp, sn, fadd_);
  const float ip = __builtin_amdgcn_rcpf(sp), in = __builtin_amdgcn_rcpf(sn);
  pp0 *= ip; pp1 *= ip; pn0 *= in; pn1 *= in;
}

DI f32x4 geglu4(f32x4 g, f32x4 u) {
  constexpr float C1 = -2.0f * 0.7978845608028654f * 1.4426950408889634f, C2 = C1 * 0.044715f;
  const f32x4 d = exp2v(g * (g * g * C2 + C1)) + 1.0f;
  const f32x4 r = {__builtin_amdgcn_rcpf(d[0]), __builtin_amdgcn_rcpf(d[1]), __builtin_amdgcn_rcpf(d[2]), __builtin_amdgcn_rcpf(d[3])};
  return (g * u) * r;
}
#define ST_AUX 0
using I0 = std::integral_constant<int, 0>; using I1 = std::integral_constant<int, 1>;

constexpr unsigned K1_X0 = 0, K1_XS = 32768, K1_W0 = 65536, K1_WS = 40960, K1_WG = 12288, K1_WU = 26624, K1_PHI = 147456, K1_PF = 149504, K1_LDS = 151552;

struct K1Dma {
  u32x4 sa, sb, sc;
  unsigned co[4], cc[2];
  unsigned lo[10];
  unsigned stab, stc;
};

template <int WN>
DI void k1_body(unsigned long long& wsum, f32x4 (&acc)[4][10], const unsigned lb, const int wm, const int lane,
                const u32x4 sx, const unsigned v0, const unsigned v1, const unsigned v2, const unsigned vxa, const unsigned vxb, const K1Dma& D) {
  const int g = lane >> 4, q = (lane & 15) >> 2, p = lane & 3, fr = lane & 15;
  const unsigned xrow = wm * 64 + fr;
  const unsigned xb0 = lb + K1_X0 + xrow * 128 + ((g ^ ((xrow >> 1) & 7)) << 4);
  const unsigned xb1 = xb0 ^ 64;
  const unsigned qb0 = lb + K1_W0 + (8 * g + q) * 192 + ((0 ^ (g & 1)) << 5) + p * 8;
  const unsigned qb1 = lb + K1_W0 + (8 * g + q) * 192 + ((1 ^ (g & 1)) << 5) + p * 8;
  const unsigned gb0 = lb + K1_W0 + K1_WG + (8 * g + 4 * (0 ^ (g & 1)) + q) * 224 + p * 8;
  const unsigned gb1 = lb + K1_W0 + K1_WG + (8 * g + 4 * (1 ^ (g & 1)) + q) * 224 + p * 8;
  const unsigned xl = lb + K1_X0 + wm * 8192, wl = lb + K1_W0;
  unsigned sxo = 128, tab = D.stab, tc = D.stc;
  int pt = 3;

  bf16x8 xf[2][4], wf[4];
  auto ldW = [&](int st, int u) -> bf16x8 {
    const int ks = u / 10, i = u % 10;
    const unsigned so = st * K1_WS;
    constexpr int NQ = WN == 0 ? 4 : 2;
    constexpr int NG = WN == 0 ? 3 : 4;
    constexpr int P0 = WN == 0 ? 4 : 0;
    if (i < NQ) {
      const int ptl = WN == 0 ? 1 + (i >> 1) : 0, nh = i & 1;
      const unsigned b = (nh ? qb1 : qb0) + so + ks * 32 * 192 + ptl * 64;
      return trrd2(b, b + 4 * 192);
    } else {
      const int j = i - NQ, up = j / NG, P = P0 + j % NG;
      const unsigned o = so + ks * 32 * 224 + up * (K1_WU - K1_WG) + P * 32;
      return trrd2(gb0 + o, gb1 + o);
    }
  };
  auto ldX = [&](int st, int ks, int mt) -> bf16x8 { return ldsrd((ks ? xb1 : xb0) + st * K1_XS + mt * 2048); };
  auto mma = [&](int u) {
    const int ks = u / 10, i = u % 10;
#pragma unroll
    for (int mt = 0; mt < 4; ++mt) {
      if (WN == 0 && (i == 2 || i == 3)) acc[mt][i] = mfma16(xf[ks][mt], wf[u & 3], acc[mt][i]);
      else acc[mt][i] = mfma16(wf[u & 3], xf[ks][mt], acc[mt][i]);
    }
  };
  auto dmaX = [&](int st, int j) { dma16(sx, xl + st * K1_XS + j * 1024, (j & 1) ? vxb : vxa, sxo + j * 16384); };
  auto dmaW = [&](int st, int s) {
    if (s < 8) dma16((s & 2) ? D.sb : D.sa, wl + st * K1_WS + D.lo[s], s < 4 ? v0 : v1, tab + D.co[s & 3]);
    else dma16(D.sc, wl + st * K1_WS + D.lo[s], v2, tc + D.cc[s - 8]);
  };
  auto warm = [&](int k) {
    int i = pt - (int)D.co[2 * k]; i = i < 0 ? 0 : i; i = i > (int)D.co[2 * k + 1] ? (int)D.co[2 * k + 1] : i;
    pfetch(k ? D.sb : D.sa, lb + K1_PF + wm * 512 + k * 256, k ? v1 : v0, (unsigned)i * (k ? D.stc : D.stab));
  };

#pragma unroll
  for (int mt = 0; mt < 4; ++mt) xf[0][mt] = ldX(0, 0, mt);
  wf[0] = ldW(0, 0); wf[1] = ldW(0, 1); wf[2] = ldW(0, 2);

  auto step = [&](auto st_, auto dox_, auto dow_) {
    constexpr int st = decltype(st_)::value;
    constexpr bool dox = decltype(dox_)::value, dow = decltype(dow_)::value;
#pragma unroll
    for (int u = 0; u < 17; ++u) {
      wf[(u + 3) & 3] = ldW(st, u + 3);
      if (u >= 2 && u < 6) xf[1][u - 2] = ldX(st, 1, u - 2);
      if (WN == 0 && dox) {
        if (u < 3) { dmaW(st ^ 1, 4 + 2 * u); dmaW(st ^ 1, 5 + 2 * u); }
        else if (u < 7) { dmaX(st ^ 1, 2 * (u - 3)); dmaX(st ^ 1, 2 * (u - 3) + 1); }
      }
      if (WN == 1 && dow && (u == 4 || u == 8)) warm(u == 8);
      mma(u);
      SB();
    }
    if (WN == 0) { sxo += 128; tab += D.stab; tc += D.stc; } else ++pt;
    mma(17);
    SB();
    if (WN == 0) WAIT_BAR_T(0); else WAIT_BAR_T(63);
    if (dox) {
#pragma unroll
      for (int mt = 0; mt < 4; ++mt) xf[0][mt] = ldX(st ^ 1, 0, mt);
      wf[0] = ldW(st ^ 1, 0); wf[1] = ldW(st ^ 1, 1);
    }
    if (WN == 0 && dow) { dmaW(st, 0); dmaW(st, 1); }
    mma(18);
    SB();
    if (dox) wf[2] = ldW(st ^ 1, 2);
    if (WN == 0 && dow) { dmaW(st, 2); dmaW(st, 3); }
    mma(19);
    SB();
  };
  for (int t2 = 0; t2 < 7; ++t2) { step(I0{}, I1{}, I1{}); step(I1{}, I1{}, I1{}); }
  step(I0{}, I1{}, I0{});
  step(I1{}, I0{}, I0{});
}

__global__ __launch_bounds__(512, 2) void k1_kernel(const uint16_t* __restrict__ x, const uint16_t* __restrict__ wqkv, const uint16_t* __restrict__ wphi,
                                                    const uint16_t* __restrict__ wg, const uint16_t* __restrict__ wu, const uint16_t* __restrict__ gatew,
                                                    const uint16_t* __restrict__ wdown, const uint16_t* __restrict__ wo, char* __restrict__ ws) {
  extern __shared__ __attribute__((aligned(16))) char smem[];
  const unsigned lb = (unsigned)(size_t)(LAS char*)smem;
  unsigned long long wsum = 0; ST(unsigned long long r0 = rt_(); unsigned long long t0 = mt_(); unsigned long long t2 = 0;)
  const int tid = threadIdx.x, lane = tid & 63;
  const int w = __builtin_amdgcn_readfirstlane(tid >> 6);
  const int wn = w >> 2, wm = w & 3;
  const int b = blockIdx.x, xj = b & 7, bi = b >> 3;
  const int c = 4 * xj + (bi & 3), rb = bi >> 2;
  const u32x4 sx = mk_srd(x, SEQ * DM * 2);
  const u32x4 sq = mk_srd(wqkv, DM * 3072 * 2), sg = mk_srd(wg, DM * DF * 2), su = mk_srd(wu, DM * DF * 2);
  f32x4 acc[4][10];
#pragma unroll
  for (int m = 0; m < 4; ++m)
#pragma unroll
    for (int i = 0; i < 10; ++i) acc[m][i] = (f32x4){0.f, 0.f, 0.f, 0.f};
  K1Dma D;
  const int m16 = rb * 16 + wm * 4;
  const auto rw = __builtin_amdgcn_make_buffer_rsrc((void*)ws, 0, 64u << 20, 0x00020000);
#define EPI_LANE() int lane = __builtin_amdgcn_mbcnt_hi(~0u, __builtin_amdgcn_mbcnt_lo(~0u, 0u)); asm volatile("" : "+v"(lane)); const int g = lane >> 4;
#define ST16(val, so) __builtin_amdgcn_raw_buffer_store_b128(__builtin_bit_cast(u32x4, val), rw, lane * 16 + (unsigned)(so), 0, ST_AUX)
  ST(unsigned long long t1 = 0;)
  if (wn == 0) {
    const unsigned vxa = (rb * 256 + 64 * wm + (lane >> 3)) * 2048 + (((lane & 7) ^ (lane >> 4)) << 4), vxb = vxa ^ 64;
    if (wm < 2) dma16(mk_srd(wphi, 32 * 32 * 32 * 2), lb + K1_PHI + wm * 1024, lane * 16, c * 2048 + wm * 1024);
#pragma unroll
    for (int j = 0; j < 8; ++j) dma16(sx, lb + K1_X0 + wm * 8192 + j * 1024, (j & 1) ? vxb : vxa, j * 16384);
    const unsigned gtbl = (c & 1) ? 0x0216543u : 0x6543210u;
    auto pat = [&](bool isq, int pi) -> unsigned {
      const int ci = 64 * pi + lane, r = (ci * (isq ? 5462 : 4682)) >> 16, cc = ci - (isq ? 12 : 14) * r;
      const int o8 = (r >> 3) & 1;
      const unsigned vq = r * 6144 + ((cc >> 2) * 1024 + 32 * c + (((cc >> 1) & 1) ^ o8) * 16 + (cc & 1) * 8) * 2;
      const unsigned vg = (r ^ (o8 << 2)) * 7168 + (112 * c + 16 * ((gtbl >> (4 * (cc >> 1))) & 7) + 8 * (cc & 1)) * 2;
      return isq ? vq : vg;
    };
    const bool q01 = wm == 3, q2 = wm >= 2;
    const unsigned v0 = pat(q01, q01 ? 0 : 2 * wm), v1 = pat(q01, q01 ? 1 : 2 * wm + 1), v2 = pat(q2, q2 ? 2 : 6);
#pragma unroll
    for (int i = 0; i < 4; ++i) { D.sa[i] = q01 ? sq[i] : sg[i]; D.sb[i] = q01 ? sq[i] : su[i]; D.sc[i] = q2 ? sq[i] : (wm ? su[i] : sg[i]); }
    D.stab = q01 ? 64 * 6144 : 64 * 7168; D.stc = q2 ? 64 * 6144 : 64 * 7168;
    D.co[0] = 0; D.co[1] = q01 ? 16 * 6144 : 32 * 7168; D.co[2] = q01 ? 32 * 6144 : 0; D.co[3] = q01 ? 48 * 6144 : 32 * 7168;
#pragma unroll
    for (int h = 0; h < 2; ++h)
#pragma unroll
      for (int i = 0; i < 4; ++i) D.lo[4 * h + i] = q01 ? h * 1024 + i * 3072 : K1_WG + (2 * wm + h) * 1024 + i * 7168;
    D.cc[0] = q2 ? (wm - 2) * 32 * 6144 : 0; D.cc[1] = D.cc[0] + (q2 ? 16 * 6144 : 32 * 7168);
    D.lo[8] = q2 ? 2048 + (wm - 2) * 6144 : K1_WG + wm * 14336 + 6144; D.lo[9] = D.lo[8] + (q2 ? 3072 : 7168);
#pragma unroll
    for (int st = 0; st < 2; ++st)
#pragma unroll
      for (int s = 0; s < (st ? 4 : 10); ++s) {
        if (s < 8) dma16((s & 2) ? D.sb : D.sa, lb + K1_W0 + st * K1_WS + D.lo[s], s < 4 ? v0 : v1, st * D.stab + D.co[s & 3]);
        else dma16(D.sc, lb + K1_W0 + st * K1_WS + D.lo[s], v2, st * D.stc + D.cc[s - 8]);
      }
    WAIT_BAR(4);
    ST(t1 = mt_();)
    k1_body<0>(wsum, acc, lb, wm, lane, sx, v0, v1, v2, vxa, vxb, D);
    ST(t2 = mt_();)
    EPI_LANE();
    const int q = (lane & 15) >> 2, p = lane & 3;
    const unsigned wpb = lb + K1_PHI + (4 * g + q) * 64 + p * 8;
    const bf16x8 wp0 = trrd2(wpb, wpb + 16 * 64), wp1 = trrd2(wpb + 32, wpb + 16 * 64 + 32);
    if (rb == 0 && wm == 0) { ST16(wp0, WS_WPF + c * 2048); ST16(wp1, WS_WPF + c * 2048 + 1024); }
    const unsigned pkb = lb + wm * 8704;
    const unsigned pw = pkb + (lane & 15) * 136 + g * 8;
#pragma unroll
    for (int mt = 0; mt < 4; ++mt) {
      f32x4 pp0, pp1, pn0, pn1;
      featmap(pack8(acc[mt][0], acc[mt][1]), wp0, wp1, pp0, pp1, pn0, pn1);
      *(LAS u32x2*)(pw + mt * 16 * 136 + 0)  = (u32x2){pk2(pp0[0], pp0[1]), pk2(pp0[2], pp0[3])};
      *(LAS u32x2*)(pw + mt * 16 * 136 + 32) = (u32x2){pk2(pp1[0], pp1[1]), pk2(pp1[2], pp1[3])};
      *(LAS u32x2*)(pw + mt * 16 * 136 + 64) = (u32x2){pk2(pn0[0], pn0[1]), pk2(pn0[2], pn0[3])};
      *(LAS u32x2*)(pw + mt * 16 * 136 + 96) = (u32x2){pk2(pn1[0], pn1[1]), pk2(pn1[2], pn1[3])};
    }
    asm volatile("s_waitcnt lgkmcnt(0)" ::: "memory");
    f32x4 kv[4][2], zz[4];
#pragma unroll
    for (int ft = 0; ft < 4; ++ft) { kv[ft][0] = (f32x4){0.f, 0.f, 0.f, 0.f}; kv[ft][1] = kv[ft][0]; zz[ft] = kv[ft][0]; }
    const bf16x8 ones = {0x3f80, 0x3f80, 0x3f80, 0x3f80, 0x3f80, 0x3f80, 0x3f80, 0x3f80};
    const unsigned pr = pkb + (4 * g + q) * 136 + p * 8;
#pragma unroll
    for (int mp = 0; mp < 2; ++mp) {
      const bf16x8 va = pack8(acc[2 * mp][2], acc[2 * mp + 1][2]), vb = pack8(acc[2 * mp][3], acc[2 * mp + 1][3]);
#pragma unroll
      for (int ft = 0; ft < 4; ++ft) {
        const bf16x8 a = trrd2(pr + (32 * mp) * 136 + ft * 32, pr + (32 * mp + 16) * 136 + ft * 32);
        kv[ft][0] = mfma16(a, va, kv[ft][0]);
        kv[ft][1] = mfma16(a, vb, kv[ft][1]);
        zz[ft] = mfma16(a, ones, zz[ft]);
      }
    }
    const unsigned rd = lb + 65536 + wm * 8192 + lane * 16;
#pragma unroll
    for (int ft = 0; ft < 4; ++ft) {
      *(LAS f32x4*)(rd + (ft * 2 + 0) * 1024) = kv[ft][0];
      *(LAS f32x4*)(rd + (ft * 2 + 1) * 1024) = kv[ft][1];
    }
    { const int ft = lane & 3;
      const f32x4 zsel = ft == 0 ? zz[0] : (ft == 1 ? zz[1] : (ft == 2 ? zz[2] : zz[3]));
      *(LAS f32x4*)(lb + 65536 + 32768 + wm * 256 + ft * 64 + g * 16) = zsel; }
    const int jp = 7 * c + ((c & 1) ? 1 : 4), js = 7 * c + ((c & 1) ? 0 : 6);
    const unsigned hp = WS_AP + ((jp >> 1) * 128 + m16) * 1024, hs = WS_AP + ((js >> 1) * 128 + m16) * 1024 + (js & 1) * 8;
#pragma unroll
    for (int mt = 0; mt < 4; ++mt) {
      const f32x4 h0 = geglu4(acc[mt][4], acc[mt][7]), h1 = geglu4(acc[mt][5], acc[mt][8]), h2 = geglu4(acc[mt][6], acc[mt][9]);
      ST16(pack8(h0, h1), hp + mt * 1024);
      __builtin_amdgcn_raw_buffer_store_b64((u32x2){pk2(h2[0], h2[1]), pk2(h2[2], h2[3])}, rw, lane * 16 + hs + mt * 1024, 0, ST_AUX);
    }
  } else {
    const u32x4 sdn = mk_srd(wdown, DF * DM * 2), swo = mk_srd(wo, DM * DM * 2);
    const int r8 = 8 * rb + ((lane >> 2) & 7), l3 = lane & 3;
    const unsigned vx_ = (rb * 256 + (c & 3) * 64 + 32 * wm + (lane >> 1)) * 2048 + (lane & 1) * 64;
    const unsigned vg_ = r8 * 7168 + ((224 * c) & ~63) + l3 * 64;
    const unsigned vq_ = r8 * 6144 + (l3 == 3 ? 0 : l3) * 2048 + 64 * c;
    const unsigned v0 = wm < 2 ? vx_ : (wm == 2 ? vg_ : vq_);
    const unsigned v1 = wm == 2 ? vg_ : (wm < 2 ? (b * 7 + 4 * wm) * 4096 : b * 2 * 4096) + lane * 64;
#pragma unroll
    for (int i = 0; i < 4; ++i) { D.sa[i] = wm < 2 ? sx[i] : (wm == 2 ? sg[i] : sq[i]); D.sb[i] = wm < 2 ? sdn[i] : (wm == 2 ? su[i] : swo[i]); }
    D.stab = wm < 2 ? 128 : (wm == 2 ? 64 * 7168 : 64 * 6144); D.stc = wm == 2 ? 64 * 7168 : 4096;
    D.co[0] = 0; D.co[1] = 15; D.co[2] = wm == 2 ? 0 : 7; D.co[3] = wm == 2 ? 15 : (wm == 3 ? 1 : 3 - wm);
#pragma unroll
    for (int t = 1; t < 3; ++t) {
      pfetch(D.sa, lb + K1_PF + wm * 512, v0, t * D.stab);
      if (wm == 2) pfetch(D.sb, lb + K1_PF + wm * 512 + 256, v1, t * D.stc);
    }
    WAIT_BAR(63);
    ST(t1 = mt_();)
    k1_body<1>(wsum, acc, lb, wm, lane, sx, v0, v1, 0u, 0u, 0u, D);
    ST(t2 = mt_();)
    EPI_LANE();
    const int grow = b * 8 + 2 * wm + (lane >> 5);
    u32x4 gx[4], gw[4];
#pragma unroll
    for (int i = 0; i < 4; ++i) {
      gx[i] = *(const u32x4*)(x + grow * DM + i * 256 + (lane & 31) * 8);
      gw[i] = *(const u32x4*)(gatew + i * 256 + (lane & 31) * 8);
    }
#pragma unroll
    for (int mt = 0; mt < 4; ++mt) ST16(pack8(acc[mt][0], acc[mt][1]), WS_QF + (c * 128 + m16 + mt) * 1024);
    const int j0 = 7 * c + ((c & 1) ? 3 : 0);
    const unsigned hp = WS_AP + ((j0 >> 1) * 128 + m16) * 1024;
#pragma unroll
    for (int mt = 0; mt < 4; ++mt) {
      const f32x4 h0 = geglu4(acc[mt][2], acc[mt][6]), h1 = geglu4(acc[mt][3], acc[mt][7]);
      ST16(pack8(h0, h1), hp + mt * 1024);
      const f32x4 h2 = geglu4(acc[mt][4], acc[mt][8]), h3 = geglu4(acc[mt][5], acc[mt][9]);
      ST16(pack8(h2, h3), hp + 131072 + mt * 1024);
    }
    {
      float s = 0.f;
#pragma unroll
      for (int i = 0; i < 4; ++i)
#pragma unroll
        for (int e = 0; e < 4; ++e) {
          const unsigned pr = pk2(__uint_as_float(gx[i][e] << 16) * __uint_as_float(gw[i][e] << 16), __uint_as_float(gx[i][e] & 0xffff0000u) * __uint_as_float(gw[i][e] & 0xffff0000u));
          s += __uint_as_float(pr << 16) + __uint_as_float(pr & 0xffff0000u);
        }
#pragma unroll
      for (int o = 1; o < 32; o <<= 1) s += __shfl_xor(s, o);
      s = bfr(s);
      const float gv = bfr(1.0f / (1.0f + __expf(-s)));
      if ((lane & 31) == 0) ((float*)(ws + WS_G))[grow] = gv;
      if (wm == 0 && lane == 0 && b < 136) ((unsigned*)(ws + WS_FLAGS))[b * 32] = 0u;
    }
  }
  LGKM_BAR();
  {
    int tid = w * 64 + __builtin_amdgcn_mbcnt_hi(~0u, __builtin_amdgcn_mbcnt_lo(~0u, 0u)); asm volatile("" : "+v"(tid));
    const unsigned rd = lb + 65536 + tid * 16;
    const f32x4 s = *(LAS f32x4*)(rd) + *(LAS f32x4*)(rd + 8192) + *(LAS f32x4*)(rd + 16384) + *(LAS f32x4*)(rd + 24576);
    *(f32x4*)(ws + WS_KVP + ((size_t)(c * 8 + rb) * 512 + tid) * 16) = s;
    if (tid < 16) {
      const unsigned rz = lb + 65536 + 32768 + tid * 16;
      const f32x4 zs = *(LAS f32x4*)(rz) + *(LAS f32x4*)(rz + 256) + *(LAS f32x4*)(rz + 512) + *(LAS f32x4*)(rz + 768);
      *(f32x4*)(ws + WS_ZP + ((size_t)(c * 8 + rb) * 16 + tid) * 16) = zs;
    }
  }
}

constexpr unsigned K2_SLOT = 32768, K2_KV = 131072, K2_LDS = 131072 + 4608;
constexpr int S_OWN = 20;
constexpr int S_ALL = 36, S_O = 28;
#define VM_BAR(N) do { asm volatile("" ::: "memory"); __builtin_amdgcn_s_waitcnt(((N) & 15) | 0xF70 | (((N) >> 4) << 14)); __builtin_amdgcn_s_barrier(); asm volatile("" ::: "memory"); } while (0)

DI void poll_ge(const unsigned* flag, unsigned need, int w, int sel) {
  unsigned v, sv, cnt;
  asm volatile("s_cmp_lg_u32 %[w], %[sel]\n\t"
               "s_cbranch_scc1 2f\n\t"
               "s_mov_b32 %[cnt], 0x400000\n\t"
               "v_mov_b32 %[v], 0\n"
               "1:\n\t"
               "global_load_dword %[v], %[v], %[p] sc1\n\t"
               "s_waitcnt vmcnt(0)\n\t"
               "v_readfirstlane_b32 %[sv], %[v]\n\t"
               "v_mov_b32 %[v], 0\n\t"
               "s_cmp_ge_u32 %[sv], %[need]\n\t"
               "s_cbranch_scc1 2f\n\t"
               "s_sleep 2\n\t"
               "s_sub_u32 %[cnt], %[cnt], 1\n\t"
               "s_cmp_lg_u32 %[cnt], 0\n\t"
               "s_cbranch_scc1 1b\n"
               "2:"
               : [v] "=&v"(v), [sv] "=&s"(sv), [cnt] "=&s"(cnt) : [p] "s"(flag), [need] "s"(need), [w] "s"(w), [sel] "s"(sel) : "memory", "scc");
}
DI void add_one(unsigned* flag, int w, int sel) {
  unsigned z, o; unsigned long long ex;
  asm volatile("s_cmp_lg_u32 %[w], %[sel]\n\t"
               "s_cbranch_scc1 2f\n\t"
               "v_mov_b32 %[z], 0\n\t"
               "v_mov_b32 %[o], 1\n\t"
               "s_mov_b64 %[ex], exec\n\t"
               "s_mov_b64 exec, 1\n\t"
               "global_atomic_add %[z], %[o], %[p]\n\t"
               "s_mov_b64 exec, %[ex]\n"
               "2:"
               : [z] "=&v"(z), [o] "=&v"(o), [ex] "=&s"(ex) : [p] "s"(flag), [w] "s"(w), [sel] "s"(sel) : "memory", "scc");
}
#define lda(d, srd, voff, soff) d = __builtin_bit_cast(bf16x8, __builtin_amdgcn_raw_buffer_load_b128(srd, voff, soff, 16))

template <bool helper>
DI void k2_body(const uint16_t* __restrict__ wdown, const uint16_t* __restrict__ wo, char* __restrict__ ws, uint16_t* __restrict__ out, const unsigned lb) {
  ST(unsigned long long wsum = 0, r0 = rt_(), t0 = mt_();)
  const int tid = threadIdx.x, lane = tid & 63;
  const int w = __builtin_amdgcn_readfirstlane(tid >> 6);
  const int g = lane >> 4, q = (lane & 15) >> 2, p = lane & 3;
  const int b = blockIdx.x, xj = b & 7, bi = b >> 3;
  const int mt128 = 4 * (xj & 3) + (bi >> 3), nt128 = bi & 7;
  const int pair = mt128 * 8 + nt128;
  constexpr int S0 = helper ? S_OWN : 0;
  unsigned* const flags = (unsigned*)(ws + WS_FLAGS);
  const int oc = 8 * (xj & 3) + nt128, op = bi >> 3;
  const int kg = w & 3, wm = w >> 2;
  const float* const gs = (const float*)(ws + WS_G);
  const auto sa = __builtin_amdgcn_make_buffer_rsrc((void*)(ws + WS_AP), 0, 144u * 131072u, 0x00020000);
  const u32x4 sd = mk_srd(wdown, DF * DM * 2), so_ = mk_srd(wo, DM * DM * 2);
  f32x4 kvl[8], zl[8]; bf16x8 wp0, wp1, qf[4]; float go[4];
  if (helper) {
    const char* kp = ws + WS_KVP + ((size_t)oc * 8 * 512 + tid) * 16;
    wp0 = *(const bf16x8*)(ws + WS_WPF + oc * 2048 + lane * 16); wp1 = *(const bf16x8*)(ws + WS_WPF + oc * 2048 + 1024 + lane * 16);
#pragma unroll
    for (int i = 0; i < 4; ++i) {
      qf[i] = *(const bf16x8*)(ws + WS_QF + ((size_t)oc * 128 + op * 32 + 4 * w + i) * 1024 + lane * 16);
      go[i] = gs[(op * 32 + 4 * w + i) * 16 + (lane & 15)];
    }
#pragma unroll
    for (int r = 0; r < 8; ++r) kvl[r] = *(const f32x4*)(kp + r * 8192);
    if (w == 7) {
      const char* zp = ws + WS_ZP + ((size_t)oc * 8 * 16 + (lane & 15)) * 16;
#pragma unroll
      for (int r = 0; r < 8; ++r) zl[r] = *(const f32x4*)(zp + r * 256);
    }
  }
  asm volatile("" ::: "memory");
  const unsigned va = lane * 16;
  const unsigned vb0 = (16 * w + g) * 2048 + nt128 * 256 + (((lane & 15) ^ (g << 1)) << 4), vb1 = (vb0 ^ 128) + 4 * 2048;
  const unsigned a_so = kg * 131072 + (mt128 * 8 + 4 * wm) * 1024;
  auto pieceB = [&](int S, int slot, int i) {
    u32x4 r;
#pragma unroll
    for (int j = 0; j < 4; ++j) r[j] = S >= S_O ? so_[j] : sd[j];
    dma16(r, lb + slot * K2_SLOT + w * 4096 + i * 1024, (i & 1) ? vb1 : vb0, (S >= S_O ? S - S_O : S) * 262144 + (i >> 1) * 16384);
  };
  bf16x8 a0[4], a1[4], a2[4], a3[4];
#pragma unroll
  for (int i = 0; i < 4; ++i) { lda(a0[i], sa, va, a_so + S0 * 524288 + i * 1024); pieceB(S0, 0, i); }
#pragma unroll
  for (int i = 0; i < 4; ++i) lda(a1[i], sa, va, a_so + (S0 + 1) * 524288 + i * 1024);
#pragma unroll
  for (int i = 0; i < 4; ++i) pieceB(S0 + 1, 1, i);
#pragma unroll
  for (int i = 0; i < 4; ++i) lda(a2[i], sa, va, a_so + (S0 + 2) * 524288 + i * 1024);
  SB();
  if (helper) {
    bf16x8 pf[4][2];
#pragma unroll
    for (int i = 0; i < 4; ++i) {
      f32x4 pp0, pp1, pn0, pn1;
      featmap(qf[i], wp0, wp1, pp0, pp1, pn0, pn1);
      pf[i][0] = pack8(pp0, pp1); pf[i][1] = pack8(pn0, pn1);
    }
    const f32x4 kvs = ((kvl[0] + kvl[1]) + (kvl[2] + kvl[3])) + ((kvl[4] + kvl[5]) + (kvl[6] + kvl[7]));
    const int ft = w >> 1, dt = w & 1;
    *(LAS u32x2*)(lb + K2_KV + (dt * 2 + (ft >> 1)) * 1024 + lane * 16 + (ft & 1) * 8) = (u32x2){pk2(kvs[0], kvs[1]), pk2(kvs[2], kvs[3])};
    if (w == 7) {
      const f32x4 zs = ((zl[0] + zl[1]) + (zl[2] + zl[3])) + ((zl[4] + zl[5]) + (zl[6] + zl[7]));
      *(LAS u32x2*)(lb + K2_KV + 4096 + ((lane >> 3) & 1) * 64 + (lane & 3) * 16 + ((lane >> 2) & 1) * 8) = (u32x2){pk2(zs[0], zs[1]), pk2(zs[2], zs[3])};
    }
    LGKM_BAR();
    bf16x8 ka[2][2], za[2];
#pragma unroll
    for (int ks = 0; ks < 2; ++ks) {
      ka[0][ks] = ldsrd(lb + K2_KV + (0 + ks) * 1024 + lane * 16);
      ka[1][ks] = ldsrd(lb + K2_KV + (2 + ks) * 1024 + lane * 16);
      za[ks] = ldsrd(lb + K2_KV + 4096 + ks * 64 + g * 16);
    }
#pragma unroll
    for (int i = 0; i < 4; ++i) {
      const f32x4 z4 = {0.f, 0.f, 0.f, 0.f};
      f32x4 n0 = mfma16(ka[0][1], pf[i][1], mfma16(ka[0][0], pf[i][0], z4));
      f32x4 n1 = mfma16(ka[1][1], pf[i][1], mfma16(ka[1][0], pf[i][0], z4));
      f32x4 dn = mfma16(za[1], pf[i][1], mfma16(za[0], pf[i][0], z4));
      const float sc = go[i] * __builtin_amdgcn_rcpf(dn[0] + 1e-6f);
      n0 *= sc; n1 *= sc;
      __builtin_amdgcn_raw_buffer_store_b128(__builtin_bit_cast(u32x4, pack8(n0, n1)), sa, lane * 16 + ((112 + oc) * 128 + op * 32 + 4 * w + i) * 1024, 0, 16  );
    }
  }
  ST(unsigned long long t1 = mt_();)
  f32x4 acc[4][8];
#pragma unroll
  for (int m = 0; m < 4; ++m)
#pragma unroll
    for (int n = 0; n < 8; ++n) acc[m][n] = (f32x4){0.f, 0.f, 0.f, 0.f};
  const unsigned bb0 = lb + (32 * kg + 4 * g + q) * 256 + (((4 * (g & 1) + q) << 1 | (p >> 1)) << 4) + (p & 1) * 8;
  bf16x8 bw[4];
  auto ldB = [&](int slot, int nt) -> bf16x8 { const unsigned a = (bb0 ^ (nt << 5)) + slot * K2_SLOT; return trrd2(a, a + 4096); };
  float gm[4];
  auto ldg = [&]() {
    const int l_ = __builtin_amdgcn_mbcnt_hi(~0u, __builtin_amdgcn_mbcnt_lo(~0u, 0u));
#pragma unroll
    for (int m = 0; m < 4; ++m) gm[m] = gs[mt128 * 128 + wm * 64 + m * 16 + (l_ & 15)];
    asm volatile("" ::: "memory");
  };
  auto scale = [&]() {
#pragma unroll
    for (int m = 0; m < 4; ++m) { const float s = bfr(1.0f - gm[m]);
#pragma unroll
      for (int n = 0; n < 8; ++n) acc[m][n] *= s; }
  };
  using I2 = std::integral_constant<int, 2>; using I3 = std::integral_constant<int, 3>; using I4 = std::integral_constant<int, 4>;
  using I12 = std::integral_constant<int, 12>;
  __builtin_amdgcn_s_waitcnt(0xC07F);
  VM_BAR(12);
  bw[0] = ldB(0, 0); bw[1] = ldB(0, 1); bw[2] = ldB(0, 2);
  auto step = [&](int S, auto sl_, bf16x8 (&ac)[4], bf16x8 (&an1)[4], bf16x8 (&an3)[4], auto isb_, auto isa_, auto wn_, auto nxt_) {
    constexpr int sl = decltype(sl_)::value;
    constexpr bool isb = decltype(isb_)::value, isa = decltype(isa_)::value, nxt = decltype(nxt_)::value;
#pragma unroll
    for (int u = 0; u < 8; ++u) {
      if (u == 6 && nxt) {
        VM_BAR(decltype(wn_)::value);
        bw[0] = ldB((sl + 1) & 3, 0); bw[1] = ldB((sl + 1) & 3, 1);
      }
      if (u < 5) bw[(u + 3) & 3] = ldB(sl, u + 3);
      if (u == 7 && nxt) bw[2] = ldB((sl + 1) & 3, 2);
      if (isb && u < 2) { pieceB(S + 2, (sl + 2) & 3, 2 * u); pieceB(S + 2, (sl + 2) & 3, 2 * u + 1); }
      if (isa && u >= 2 && u < 6) lda(an3[u - 2], sa, va, a_so + (S + 3) * 524288 + (u - 2) * 1024);
#pragma unroll
      for (int m = 0; m < 4; ++m) acc[m][u] = mfma16(bw[u & 3], ac[m], acc[m][u]);
      SB();
    }
  };
  const auto rp = __builtin_amdgcn_make_buffer_rsrc((void*)(ws + WS_PART), 0, 128u * 65536u, 0x00020000);
  const unsigned po = pair * 65536 + w * 8192;
  u32x4 part[8];
  if (helper) {
#pragma unroll 1
    for (int S = S_OWN; S < S_O; S += 4) {
      step(S, I0{}, a0, a1, a3, I1{}, I1{}, I12{}, I1{});
      step(S + 1, I1{}, a1, a2, a0, I1{}, I1{}, I12{}, I1{});
      add_one(flags + op * 32, w, 4 * (S - S_OWN));
      step(S + 2, I2{}, a2, a3, a1, I1{}, I1{}, I12{}, I1{});
      poll_ge(flags + (mt128 >> 2) * 32, 32u, w, 7 + S - S_OWN);
      step(S + 3, I3{}, a3, a0, a2, I1{}, I1{}, I12{}, I1{});
    }
    ldg();
    scale();
    int nq = 1; asm volatile("" : "+s"(nq));
#pragma unroll 1
    for (int i = 0; i < nq; ++i) {
      step(28, I0{}, a0, a1, a3, I1{}, I1{}, I12{}, I1{});
      step(29, I1{}, a1, a2, a0, I1{}, I1{}, I12{}, I1{});
      step(30, I2{}, a2, a3, a1, I1{}, I1{}, I12{}, I1{});
      step(31, I3{}, a3, a0, a2, I1{}, I1{}, I12{}, I1{});
    }
    ST(wsum = mt_();)
    step(32, I0{}, a0, a1, a3, I1{}, I1{}, I12{}, I1{});
    step(33, I1{}, a1, a2, a0, I1{}, I0{}, I4{}, I1{});
    step(34, I2{}, a2, a3, a1, I0{}, I0{}, I0{}, I1{});
    step(35, I3{}, a3, a0, a2, I0{}, I0{}, I0{}, I0{});
#pragma unroll
    for (int i = 0; i < 8; ++i) part[i] = (u32x4){0u, 0u, 0u, 0u};
  } else {
#pragma unroll 1
    for (int S = 0; S < S_OWN - 4; S += 4) {
      step(S, I0{}, a0, a1, a3, I1{}, I1{}, I12{}, I1{});
      step(S + 1, I1{}, a1, a2, a0, I1{}, I1{}, I12{}, I1{});
      step(S + 2, I2{}, a2, a3, a1, I1{}, I1{}, I12{}, I1{});
      step(S + 3, I3{}, a3, a0, a2, I1{}, I1{}, I12{}, I1{});
    }
    ST(wsum = mt_();)
    gm[0] = gs[mt128 * 128 + wm * 64 + (2 * (kg & 1) + (kg >> 1)) * 16 + (lane & 15)];
    step(S_OWN - 4, I0{}, a0, a1, a3, I1{}, I1{}, I12{}, I1{});
    step(S_OWN - 3, I1{}, a1, a2, a0, I1{}, I0{}, I4{}, I1{});
    poll_ge(flags + (8 + pair) * 32, 1u, w, 7);
    step(S_OWN - 2, I2{}, a2, a3, a1, I0{}, I0{}, I0{}, I1{});
#pragma unroll
    for (int i = 0; i < 8; ++i) part[i] = __builtin_amdgcn_raw_buffer_load_b128(rp, va, po + i * 1024, 16);
    step(S_OWN - 1, I3{}, a3, a0, a2, I0{}, I0{}, I0{}, I0{});
  }
  ST(unsigned long long t2 = mt_(), e0 = 0, e1 = 0, e2 = 0;)
  {
  int lane = __builtin_amdgcn_mbcnt_hi(~0u, __builtin_amdgcn_mbcnt_lo(~0u, 0u)); asm volatile("" : "+v"(lane));
  const int g = lane >> 4;
  f32x4 fin[8];
  const unsigned own = lb + w * 16384 + lane * 16;
  auto reduce = [&](auto k0_, auto k1_) {
    constexpr int k0 = decltype(k0_)::value, k1 = decltype(k1_)::value;
    LGKM_BAR();
    ST(e0 = mt_();)
#pragma unroll
    for (int n = 0; n < 8; ++n) { *(LAS f32x4*)((own ^ 16384) + n * 1024) = acc[2 - 2 * k0][n]; *(LAS f32x4*)((own ^ 16384) + 8192 + n * 1024) = acc[3 - 2 * k0][n]; }
    LGKM_BAR();
    ST(e1 = mt_();)
    f32x4 snd[8];
#pragma unroll
    for (int n = 0; n < 8; ++n) {
      snd[n] = acc[2 * k0 + 1 - k1][n] + *(LAS f32x4*)(own + (1 - k1) * 8192 + n * 1024);
      fin[n] = acc[2 * k0 + k1][n] + *(LAS f32x4*)(own + k1 * 8192 + n * 1024);
    }
#pragma unroll
    for (int n = 0; n < 8; ++n) *(LAS f32x4*)(own + (1 - k1) * 8192 + n * 1024) = snd[n];
    LGKM_BAR();
    ST(e2 = mt_();)
#pragma unroll
    for (int n = 0; n < 8; ++n) fin[n] += *(LAS f32x4*)((own ^ 32768) + k1 * 8192 + n * 1024);
  };
  if (kg == 0) reduce(I0{}, I0{}); else if (kg == 1) reduce(I1{}, I0{}); else if (kg == 2) reduce(I0{}, I1{}); else reduce(I1{}, I1{});
  if (helper) {
#pragma unroll
    for (int n = 0; n < 8; ++n) __builtin_amdgcn_raw_buffer_store_b128(__builtin_bit_cast(u32x4, fin[n]), rp, lane * 16 + po + n * 1024, 0, 16);
    WAIT_BAR(0);
    if (w == 0 && lane == 0) __hip_atomic_store((GAS unsigned*)(flags + (8 + pair) * 32), 1u, __ATOMIC_RELAXED, __HIP_MEMORY_SCOPE_AGENT);
  } else {
    const float omg = bfr(1.0f - gm[0]);
    uint16_t* orow = out + (size_t)(mt128 * 128 + wm * 64 + (2 * (kg & 1) + (kg >> 1)) * 16 + (lane & 15)) * DM + nt128 * 128 + 4 * g;
#pragma unroll
    for (int n = 0; n < 8; ++n) {
      const f32x4 v = fin[n] * omg + __builtin_bit_cast(f32x4, part[n]);
      *(u32x2*)(orow + n * 16) = (u32x2){pk2(v[0], v[1]), pk2(v[2], v[3])};
    }
  }
  }
}

__global__ __launch_bounds__(512, 2) void k2_kernel(const uint16_t* __restrict__ wdown, const uint16_t* __restrict__ wo, char* __restrict__ ws,
                                                    uint16_t* __restrict__ out) {
  extern __shared__ __attribute__((aligned(16))) char smem[];
  const unsigned lb = (unsigned)(size_t)(LAS char*)smem;
  if (blockIdx.x & 4) k2_body<true>(wdown, wo, ws, out, lb);
  else k2_body<false>(wdown, wo, ws, out, lb);
}

extern "C" void kernel_launch(void* const* d_in, const int* in_sizes, int n_in, void* d_out, int out_size, void* d_ws, size_t ws_size,
                              hipStream_t stream) {
  const uint16_t* x = (const uint16_t*)d_in[0];
  const uint16_t* wqkv = (const uint16_t*)d_in[1];
  const uint16_t* wphi = (const uint16_t*)d_in[2];
  const uint16_t* wo = (const uint16_t*)d_in[3];
  const uint16_t* wg = (const uint16_t*)d_in[5];
  const uint16_t* wu = (const uint16_t*)d_in[6];
  const uint16_t* wd = (const uint16_t*)d_in[7];
  const uint16_t* gw = (const uint16_t*)d_in[8];
  (void)hipFuncSetAttribute((const void*)k1_kernel, hipFuncAttributeMaxDynamicSharedMemorySize, K1_LDS);
  (void)hipFuncSetAttribute((const void*)k2_kernel, hipFuncAttributeMaxDynamicSharedMemorySize, K2_LDS);
  k1_kernel<<<256, 512, K1_LDS, stream>>>(x, wqkv, wphi, wg, wu, gw, wd, wo, (char*)d_ws);
  k2_kernel<<<256, 512, K2_LDS, stream>>>(wd, wo, (char*)d_ws, (uint16_t*)d_out);
}
```

```cpp
#include <hip/hip_runtime.h>
#include <hip/hip_bf16.h>
#include <stdint.h>
#include <type_traits>
#pragma clang diagnostic ignored "-Wint-to-pointer-cast"

typedef short bf16x8 __attribute__((ext_vector_type(8)));
typedef short s16x4 __attribute__((ext_vector_type(4)));
typedef float f32x4 __attribute__((ext_vector_type(4)));
typedef float f32x2 __attribute__((ext_vector_type(2)));
typedef unsigned u32x4 __attribute__((ext_vector_type(4)));
typedef unsigned u32x2 __attribute__((ext_vector_type(2)));
typedef __bf16 bf16x2_t __attribute__((ext_vector_type(2)));
#define LAS __attribute__((address_space(3)))
#define GAS __attribute__((address_space(1)))
#define DI __device__ __forceinline__

constexpr int SEQ = 2048, DM = 1024, DF = 3584;
constexpr size_t WS_FLAGS = 0;
constexpr size_t WS_G     = 32768;
constexpr size_t WS_ZP    = 65536;
constexpr size_t WS_KVP   = 262144;
constexpr size_t WS_WPF   = 3u << 20;
constexpr size_t WS_QF    = 4u << 20;
constexpr size_t WS_AP    = 16u << 20;
constexpr size_t WS_PART  = 40u << 20;

#define ABL 0
DI f32x4 mfma16(bf16x8 a, bf16x8 b, f32x4 c) { return __builtin_amdgcn_mfma_f32_16x16x32_bf16(a, b, c, 0, 0, 0); }
DI s16x4 trrd(unsigned a) { return __builtin_amdgcn_ds_read_tr16_b64_v4i16((LAS s16x4*)a); }
DI bf16x8 trrd2(unsigned a0, unsigned a1) { s16x4 lo = trrd(a0), hi = trrd(a1); return __builtin_shufflevector(lo, hi, 0, 1, 2, 3, 4, 5, 6, 7); }
DI bf16x8 ldsrd(unsigned a) { return *(LAS bf16x8*)a; }
DI unsigned pk2(float lo, float hi) { f32x2 v = {lo, hi}; bf16x2_t b = __builtin_convertvector(v, bf16x2_t); return __builtin_bit_cast(unsigned, b); }
DI bf16x8 pack8(f32x4 a, f32x4 b) { u32x4 r = {pk2(a[0], a[1]), pk2(a[2], a[3]), pk2(b[0], b[1]), pk2(b[2], b[3])}; return __builtin_bit_cast(bf16x8, r); }
DI float bfr(float x) { unsigned u = pk2(x, x); return __uint_as_float(u << 16); }
DI u32x4 mk_srd(const void* p, unsigned bytes) {
  uint64_t a = (uint64_t)p;
  u32x4 r;
  r[0] = __builtin_amdgcn_readfirstlane((unsigned)a);
  r[1] = __builtin_amdgcn_readfirstlane((unsigned)(a >> 32) & 0xffffu);
  r[2] = __builtin_amdgcn_readfirstlane(bytes);
  r[3] = 0x00020000u;
  return r;
}
DI void pfetch(u32x4 srd, unsigned lds, unsigned voff, unsigned soff) {
  asm volatile("s_mov_b32 m0, %0\n\ts_nop 0\n\tbuffer_load_dword %1, %2, %3 offen lds" ::"s"(lds), "v"(voff), "s"(srd), "s"(soff) : "memory");
}
DI void dma16(u32x4 srd, unsigned lds, unsigned voff, unsigned soff) {
  asm volatile("s_mov_b32 m0, %0\n\ts_nop 0\n\tbuffer_load_dwordx4 %1, %2, %3 offen lds" ::"s"(lds), "v"(voff), "s"(srd), "s"(soff) : "memory");
}
#define WAIT_BAR(N) do { asm volatile("" ::: "memory"); __builtin_amdgcn_s_waitcnt(((N) & 15) | 0x70 | (((N) >> 4) << 14)); __builtin_amdgcn_s_barrier(); asm volatile("" ::: "memory"); } while (0)
#define LGKM_BAR() do { asm volatile("" ::: "memory"); __builtin_amdgcn_s_waitcnt(0xC07F); __builtin_amdgcn_s_barrier(); asm volatile("" ::: "memory"); } while (0)
#define SB() __builtin_amdgcn_sched_barrier(0)
#define ST(...)
#define WAIT_BAR_T(N) WAIT_BAR(N)

DI float fmx(float a, float b) { return __builtin_amdgcn_fmed3f(a, b, __builtin_inff()); }
#define XRED2(a, b, OP) do { \
    auto r1_ = __builtin_amdgcn_permlane16_swap(__float_as_uint(a), __float_as_uint(b), false, false); \
    float m_ = OP(__uint_as_float(r1_[0]), __uint_as_float(r1_[1])); \
    auto r2_ = __builtin_amdgcn_permlane32_swap(__float_as_uint(m_), __float_as_uint(m_), false, false); \
    m_ = OP(__uint_as_float(r2_[0]), __uint_as_float(r2_[1])); \
    auto r3_ = __builtin_amdgcn_permlane16_swap(__float_as_uint(m_), __float_as_uint(m_), false, false); \
    a = __uint_as_float(r3_[0]); b = __uint_as_float(r3_[1]); } while (0)
DI float fadd_(float a, float b) { return a + b; }
DI f32x4 exp2v(f32x4 v) { return (f32x4){__builtin_amdgcn_exp2f(v[0]), __builtin_amdgcn_exp2f(v[1]), __builtin_amdgcn_exp2f(v[2]), __builtin_amdgcn_exp2f(v[3])}; }

DI void featmap(bf16x8 fr, bf16x8 wp0, bf16x8 wp1, f32x4& pp0, f32x4& pp1, f32x4& pn0, f32x4& pn1) {
  const f32x4 z4 = {0.f, 0.f, 0.f, 0.f};
  constexpr float L2E = 1.4426950408889634f;
  const f32x4 t0 = mfma16(wp0, fr, z4) * L2E, t1 = mfma16(wp1, fr, z4) * L2E;
  float mx = fmaxf(fmaxf(fmaxf(t0[0], t0[1]), fmaxf(t0[2], t0[3])), fmaxf(fmaxf(t1[0], t1[1]), fmaxf(t1[2], t1[3])));
  float nm = -fminf(fminf(fminf(t0[0], t0[1]), fminf(t0[2], t0[3])), fminf(fminf(t1[0], t1[1]), fminf(t1[2], t1[3])));
  XRED2(mx, nm, fmx);
  pp0 = exp2v(t0 - mx); pp1 = exp2v(t1 - mx);
  pn0 = exp2v(-nm - t0); pn1 = exp2v(-nm - t1);
  const f32x4 s4p = pp0 + pp1, s4n = pn0 + pn1;
  float sp = (s4p[0] + s4p[1]) + (s4p[2] + s4p[3]), sn = (s4n[0] + s4n[1]) + (s4n[2] + s4n[3]);
  XRED2(sp, sn, fadd_);
  const float ip = __builtin_amdgcn_rcpf(sp), in = __builtin_amdgcn_rcpf(sn);
  pp0 *= ip; pp1 *= ip; pn0 *= in; pn1 *= in;
}

DI f32x4 geglu4(f32x4 g, f32x4 u) {
  constexpr float C1 = -2.0f * 0.7978845608028654f * 1.4426950408889634f, C2 = C1 * 0.044715f;
  const f32x4 d = exp2v(g * (g * g * C2 + C1)) + 1.0f;
  const f32x4 r = {__builtin_amdgcn_rcpf(d[0]), __builtin_amdgcn_rcpf(d[1]), __builtin_amdgcn_rcpf(d[2]), __builtin_amdgcn_rcpf(d[3])};
  return (g * u) * r;
}
#define ST_AUX 16
using I0 = std::integral_constant<int, 0>; using I1 = std::integral_constant<int, 1>;

constexpr unsigned K1_X0 = 0, K1_XS = 32768, K1_W0 = 65536, K1_WS = 40960, K1_WG = 12288, K1_WU = 26624, K1_PHI = 147456, K1_PF = 149504, K1_LDS = 151552;

struct K1Dma {
  u32x4 sa, sb, sc;
  unsigned co[4], cc[2];
  unsigned lo[10];
  unsigned stab, stc;
};

template <int WN>
DI void k1_body(unsigned long long& wsum, f32x4 (&acc)[4][10], const unsigned lb, const int wm, const int lane,
                const u32x4 sx, const unsigned v0, const unsigned v1, const unsigned v2, const unsigned vxa, const unsigned vxb, const K1Dma& D) {
  const int g = lane >> 4, q = (lane & 15) >> 2, p = lane & 3, fr = lane & 15;
  const unsigned xrow = wm * 64 + fr;
  const unsigned xb0 = lb + K1_X0 + xrow * 128 + ((g ^ ((xrow >> 1) & 7)) << 4);
  const unsigned xb1 = xb0 ^ 64;
  const unsigned qb0 = lb + K1_W0 + (8 * g + q) * 192 + ((0 ^ (g & 1)) << 5) + p * 8;
  const unsigned qb1 = lb + K1_W0 + (8 * g + q) * 192 + ((1 ^ (g & 1)) << 5) + p * 8;
  const unsigned gb0 = lb + K1_W0 + K1_WG + (8 * g + 4 * (0 ^ (g & 1)) + q) * 224 + p * 8;
  const unsigned gb1 = lb + K1_W0 + K1_WG + (8 * g + 4 * (1 ^ (g & 1)) + q) * 224 + p * 8;
  const unsigned xl = lb + K1_X0 + wm * 8192, wl = lb + K1_W0;
  unsigned sxo = 128, tab = D.stab, tc = D.stc;
  int pt = 3;

  bf16x8 xf[2][4], wf[4];
  auto ldW = [&](int st, int u) -> bf16x8 {
    const int ks = u / 10, i = u % 10;
    const unsigned so = st * K1_WS;
    constexpr int NQ = WN == 0 ? 4 : 2;
    constexpr int NG = WN == 0 ? 3 : 4;
    constexpr int P0 = WN == 0 ? 4 : 0;
    if (i < NQ) {
      const int ptl = WN == 0 ? 1 + (i >> 1) : 0, nh = i & 1;
      const unsigned b = (nh ? qb1 : qb0) + so + ks * 32 * 192 + ptl * 64;
      return trrd2(b, b + 4 * 192);
    } else {
      const int j = i - NQ, up = j / NG, P = P0 + j % NG;
      const unsigned o = so + ks * 32 * 224 + up * (K1_WU - K1_WG) + P * 32;
      return trrd2(gb0 + o, gb1 + o);
    }
  };
  auto ldX = [&](int st, int ks, int mt) -> bf16x8 { return ldsrd((ks ? xb1 : xb0) + st * K1_XS + mt * 2048); };
  auto mma = [&](int u) {
    const int ks = u / 10, i = u % 10;
#pragma unroll
    for (int mt = 0; mt < 4; ++mt) {
      if (WN == 0 && (i == 2 || i == 3)) acc[mt][i] = mfma16(xf[ks][mt], wf[u & 3], acc[mt][i]);
      else acc[mt][i] = mfma16(wf[u & 3], xf[ks][mt], acc[mt][i]);
    }
  };
  auto dmaX = [&](int st, int j) { dma16(sx, xl + st * K1_XS + j * 1024, (j & 1) ? vxb : vxa, sxo + j * 16384); };
  auto dmaW = [&](int st, int s) {
    if (s < 8) dma16((s & 2) ? D.sb : D.sa, wl + st * K1_WS + D.lo[s], s < 4 ? v0 : v1, tab + D.co[s & 3]);
    else dma16(D.sc, wl + st * K1_WS + D.lo[s], v2, tc + D.cc[s - 8]);
  };
  auto warm = [&](int k) {
    int i = pt - (int)D.co[2 * k]; i = i < 0 ? 0 : i; i = i > (int)D.co[2 * k + 1] ? (int)D.co[2 * k + 1] : i;
    pfetch(k ? D.sb : D.sa, lb + K1_PF + wm * 512 + k * 256, k ? v1 : v0, (unsigned)i * (k ? D.stc : D.stab));
  };

#pragma unroll
  for (int mt = 0; mt < 4; ++mt) xf[0][mt] = ldX(0, 0, mt);
  wf[0] = ldW(0, 0); wf[1] = ldW(0, 1); wf[2] = ldW(0, 2);

  auto step = [&](auto st_, auto dox_, auto dow_) {
    constexpr int st = decltype(st_)::value;
    constexpr bool dox = decltype(dox_)::value, dow = decltype(dow_)::value;
#pragma unroll
    for (int u = 0; u < 17; ++u) {
      wf[(u + 3) & 3] = ldW(st, u + 3);
      if (u >= 2 && u < 6) xf[1][u - 2] = ldX(st, 1, u - 2);
      if (WN == 0 && dox) {
        if (u < 3) { dmaW(st ^ 1, 4 + 2 * u); dmaW(st ^ 1, 5 + 2 * u); }
        else if (u < 7) { dmaX(st ^ 1, 2 * (u - 3)); dmaX(st ^ 1, 2 * (u - 3) + 1); }
      }
      if (WN == 1 && dow && (u == 4 || u == 8)) warm(u == 8);
      mma(u);
      SB();
    }
    if (WN == 0) { sxo += 128; tab += D.stab; tc += D.stc; } else ++pt;
    mma(17);
    SB();
    if (WN == 0) WAIT_BAR_T(0); else WAIT_BAR_T(63);
    if (dox) {
#pragma unroll
      for (int mt = 0; mt < 4; ++mt) xf[0][mt] = ldX(st ^ 1, 0, mt);
      wf[0] = ldW(st ^ 1, 0); wf[1] = ldW(st ^ 1, 1);
    }
    if (WN == 0 && dow) { dmaW(st, 0); dmaW(st, 1); }
    mma(18);
    SB();
    if (dox) wf[2] = ldW(st ^ 1, 2);
    if (WN == 0 && dow) { dmaW(st, 2); dmaW(st, 3); }
    mma(19);
    SB();
  };
  for (int t2 = 0; t2 < 7; ++t2) { step(I0{}, I1{}, I1{}); step(I1{}, I1{}, I1{}); }
  step(I0{}, I1{}, I0{});
  step(I1{}, I0{}, I0{});
}

__global__ __launch_bounds__(512, 2) void k1_kernel(const uint16_t* __restrict__ x, const uint16_t* __restrict__ wqkv, const uint16_t* __restrict__ wphi,
                                                    const uint16_t* __restrict__ wg, const uint16_t* __restrict__ wu, const uint16_t* __restrict__ gatew,
                                                    const uint16_t* __restrict__ wdown, const uint16_t* __restrict__ wo, char* __restrict__ ws) {
  extern __shared__ __attribute__((aligned(16))) char smem[];
  const unsigned lb = (unsigned)(size_t)(LAS char*)smem;
  unsigned long long wsum = 0; ST(unsigned long long r0 = rt_(); unsigned long long t0 = mt_(); unsigned long long t2 = 0;)
  const int tid = threadIdx.x, lane = tid & 63;
  const int w = __builtin_amdgcn_readfirstlane(tid >> 6);
  const int wn = w >> 2, wm = w & 3;
  const int b = blockIdx.x, xj = b & 7, bi = b >> 3;
  const int c = 4 * xj + (bi & 3), rb = bi >> 2;
  const u32x4 sx = mk_srd(x, SEQ * DM * 2);
  const u32x4 sq = mk_srd(wqkv, DM * 3072 * 2), sg = mk_srd(wg, DM * DF * 2), su = mk_srd(wu, DM * DF * 2);
  f32x4 acc[4][10];
#pragma unroll
  for (int m = 0; m < 4; ++m)
#pragma unroll
    for (int i = 0; i < 10; ++i) acc[m][i] = (f32x4){0.f, 0.f, 0.f, 0.f};
  K1Dma D;
  const int m16 = rb * 16 + wm * 4;
  const auto rw = __builtin_amdgcn_make_buffer_rsrc((void*)ws, 0, 64u << 20, 0x00020000);
#define EPI_LANE() int lane = __builtin_amdgcn_mbcnt_hi(~0u, __builtin_amdgcn_mbcnt_lo(~0u, 0u)); asm volatile("" : "+v"(lane)); const int g = lane >> 4;
#define ST16(val, so) __builtin_amdgcn_raw_buffer_store_b128(__builtin_bit_cast(u32x4, val), rw, lane * 16 + (unsigned)(so), 0, ST_AUX)
  ST(unsigned long long t1 = 0;)
  if (wn == 0) {
    const unsigned vxa = (rb * 256 + 64 * wm + (lane >> 3)) * 2048 + (((lane & 7) ^ (lane >> 4)) << 4), vxb = vxa ^ 64;
    if (wm < 2) dma16(mk_srd(wphi, 32 * 32 * 32 * 2), lb + K1_PHI + wm * 1024, lane * 16, c * 2048 + wm * 1024);
#pragma unroll
    for (int j = 0; j < 8; ++j) dma16(sx, lb + K1_X0 + wm * 8192 + j * 1024, (j & 1) ? vxb : vxa, j * 16384);
    const unsigned gtbl = (c & 1) ? 0x0216543u : 0x6543210u;
    auto pat = [&](bool isq, int pi) -> unsigned {
      const int ci = 64 * pi + lane, r = (ci * (isq ? 5462 : 4682)) >> 16, cc = ci - (isq ? 12 : 14) * r;
      const int o8 = (r >> 3) & 1;
      const unsigned vq = r * 6144 + ((cc >> 2) * 1024 + 32 * c + (((cc >> 1) & 1) ^ o8) * 16 + (cc & 1) * 8) * 2;
      const unsigned vg = (r ^ (o8 << 2)) * 7168 + (112 * c + 16 * ((gtbl >> (4 * (cc >> 1))) & 7) + 8 * (cc & 1)) * 2;
      return isq ? vq : vg;
    };
    const bool q01 = wm == 3, q2 = wm >= 2;
    const unsigned v0 = pat(q01, q01 ? 0 : 2 * wm), v1 = pat(q01, q01 ? 1 : 2 * wm + 1), v2 = pat(q2, q2 ? 2 : 6);
#pragma unroll
    for (int i = 0; i < 4; ++i) { D.sa[i] = q01 ? sq[i] : sg[i]; D.sb[i] = q01 ? sq[i] : su[i]; D.sc[i] = q2 ? sq[i] : (wm ? su[i] : sg[i]); }
    D.stab = q01 ? 64 * 6144 : 64 * 7168; D.stc = q2 ? 64 * 6144 : 64 * 7168;
    D.co[0] = 0; D.co[1] = q01 ? 16 * 6144 : 32 * 7168; D.co[2] = q01 ? 32 * 6144 : 0; D.co[3] = q01 ? 48 * 6144 : 32 * 7168;
#pragma unroll
    for (int h = 0; h < 2; ++h)
#pragma unroll
      for (int i = 0; i < 4; ++i) D.lo[4 * h + i] = q01 ? h * 1024 + i * 3072 : K1_WG + (2 * wm + h) * 1024 + i * 7168;
    D.cc[0] = q2 ? (wm - 2) * 32 * 6144 : 0; D.cc[1] = D.cc[0] + (q2 ? 16 * 6144 : 32 * 7168);
    D.lo[8] = q2 ? 2048 + (wm - 2) * 6144 : K1_WG + wm * 14336 + 6144; D.lo[9] = D.lo[8] + (q2 ? 3072 : 7168);
#pragma unroll
    for (int st = 0; st < 2; ++st)
#pragma unroll
      for (int s = 0; s < (st ? 4 : 10); ++s) {
        if (s < 8) dma16((s & 2) ? D.sb : D.sa, lb + K1_W0 + st * K1_WS + D.lo[s], s < 4 ? v0 : v1, st * D.stab + D.co[s & 3]);
        else dma16(D.sc, lb + K1_W0 + st * K1_WS + D.lo[s], v2, st * D.stc + D.cc[s - 8]);
      }
    WAIT_BAR(4);
    ST(t1 = mt_();)
    k1_body<0>(wsum, acc, lb, wm, lane, sx, v0, v1, v2, vxa, vxb, D);
    ST(t2 = mt_();)
    EPI_LANE();
    const int q = (lane & 15) >> 2, p = lane & 3;
    const unsigned wpb = lb + K1_PHI + (4 * g + q) * 64 + p * 8;
    const bf16x8 wp0 = trrd2(wpb, wpb + 16 * 64), wp1 = trrd2(wpb + 32, wpb + 16 * 64 + 32);
    if (rb == 0 && wm == 0) { ST16(wp0, WS_WPF + c * 2048); ST16(wp1, WS_WPF + c * 2048 + 1024); }
    const unsigned pkb = lb + wm * 8704;
    const unsigned pw = pkb + (lane & 15) * 136 + g * 8;
#pragma unroll
    for (int mt = 0; mt < 4; ++mt) {
      f32x4 pp0, pp1, pn0, pn1;
      featmap(pack8(acc[mt][0], acc[mt][1]), wp0, wp1, pp0, pp1, pn0, pn1);
      *(LAS u32x2*)(pw + mt * 16 * 136 + 0)  = (u32x2){pk2(pp0[0], pp0[1]), pk2(pp0[2], pp0[3])};
      *(LAS u32x2*)(pw + mt * 16 * 136 + 32) = (u32x2){pk2(pp1[0], pp1[1]), pk2(pp1[2], pp1[3])};
      *(LAS u32x2*)(pw + mt * 16 * 136 + 64) = (u32x2){pk2(pn0[0], pn0[1]), pk2(pn0[2], pn0[3])};
      *(LAS u32x2*)(pw + mt * 16 * 136 + 96) = (u32x2){pk2(pn1[0], pn1[1]), pk2(pn1[2], pn1[3])};
    }
    asm volatile("s_waitcnt lgkmcnt(0)" ::: "memory");
    f32x4 kv[4][2], zz[4];
#pragma unroll
    for (int ft = 0; ft < 4; ++ft) { kv[ft][0] = (f32x4){0.f, 0.f, 0.f, 0.f}; kv[ft][1] = kv[ft][0]; zz[ft] = kv[ft][0]; }
    const bf16x8 ones = {0x3f80, 0x3f80, 0x3f80, 0x3f80, 0x3f80, 0x3f80, 0x3f80, 0x3f80};
    const unsigned pr = pkb + (4 * g + q) * 136 + p * 8;
#pragma unroll
    for (int mp = 0; mp < 2; ++mp) {
      const bf16x8 va = pack8(acc[2 * mp][2], acc[2 * mp + 1][2]), vb = pack8(acc[2 * mp][3], acc[2 * mp + 1][3]);
#pragma unroll
      for (int ft = 0; ft < 4; ++ft) {
        const bf16x8 a = trrd2(pr + (32 * mp) * 136 + ft * 32, pr + (32 * mp + 16) * 136 + ft * 32);
        kv[ft][0] = mfma16(a, va, kv[ft][0]);
        kv[ft][1] = mfma16(a, vb, kv[ft][1]);
        zz[ft] = mfma16(a, ones, zz[ft]);
      }
    }
    const unsigned rd = lb + 65536 + wm * 8192 + lane * 16;
#pragma unroll
    for (int ft = 0; ft < 4; ++ft) {
      *(LAS f32x4*)(rd + (ft * 2 + 0) * 1024) = kv[ft][0];
      *(LAS f32x4*)(rd + (ft * 2 + 1) * 1024) = kv[ft][1];
    }
    { const int ft = lane & 3;
      const f32x4 zsel = ft == 0 ? zz[0] : (ft == 1 ? zz[1] : (ft == 2 ? zz[2] : zz[3]));
      *(LAS f32x4*)(lb + 65536 + 32768 + wm * 256 + ft * 64 + g * 16) = zsel; }
    const int jp = 7 * c + ((c & 1) ? 1 : 4), js = 7 * c + ((c & 1) ? 0 : 6);
    const unsigned hp = WS_AP + ((jp >> 1) * 128 + m16) * 1024, hs = WS_AP + ((js >> 1) * 128 + m16) * 1024 + (js & 1) * 8;
#pragma unroll
    for (int mt = 0; mt < 4; ++mt) {
      const f32x4 h0 = geglu4(acc[mt][4], acc[mt][7]), h1 = geglu4(acc[mt][5], acc[mt][8]), h2 = geglu4(acc[mt][6], acc[mt][9]);
      ST16(pack8(h0, h1), hp + mt * 1024);
      __builtin_amdgcn_raw_buffer_store_b64((u32x2){pk2(h2[0], h2[1]), pk2(h2[2], h2[3])}, rw, lane * 16 + hs + mt * 1024, 0, ST_AUX);
    }
  } else {
    const u32x4 sdn = mk_srd(wdown, DF * DM * 2), swo = mk_srd(wo, DM * DM * 2);
    const int r8 = 8 * rb + ((lane >> 2) & 7), l3 = lane & 3;
    const unsigned vx_ = (rb * 256 + (c & 3) * 64 + 32 * wm + (lane >> 1)) * 2048 + (lane & 1) * 64;
    const unsigned vg_ = r8 * 7168 + ((224 * c) & ~63) + l3 * 64;
    const unsigned vq_ = r8 * 6144 + (l3 == 3 ? 0 : l3) * 2048 + 64 * c;
    const unsigned v0 = wm < 2 ? vx_ : (wm == 2 ? vg_ : vq_);
    const unsigned v1 = wm == 2 ? vg_ : (wm < 2 ? (b * 7 + 4 * wm) * 4096 : b * 2 * 4096) + lane * 64;
#pragma unroll
    for (int i = 0; i < 4; ++i) { D.sa[i] = wm < 2 ? sx[i] : (wm == 2 ? sg[i] : sq[i]); D.sb[i] = wm < 2 ? sdn[i] : (wm == 2 ? su[i] : swo[i]); }
    D.stab = wm < 2 ? 128 : (wm == 2 ? 64 * 7168 : 64 * 6144); D.stc = wm == 2 ? 64 * 7168 : 4096;
    D.co[0] = 0; D.co[1] = 15; D.co[2] = wm == 2 ? 0 : 7; D.co[3] = wm == 2 ? 15 : (wm == 3 ? 1 : 3 - wm);
#pragma unroll
    for (int t = 1; t < 3; ++t) {
      pfetch(D.sa, lb + K1_PF + wm * 512, v0, t * D.stab);
      if (wm == 2) pfetch(D.sb, lb + K1_PF + wm * 512 + 256, v1, t * D.stc);
    }
    WAIT_BAR(63);
    ST(t1 = mt_();)
    k1_body<1>(wsum, acc, lb, wm, lane, sx, v0, v1, 0u, 0u, 0u, D);
    ST(t2 = mt_();)
    EPI_LANE();
    const int grow = b * 8 + 2 * wm + (lane >> 5);
    u32x4 gx[4], gw[4];
#pragma unroll
    for (int i = 0; i < 4; ++i) {
      gx[i] = *(const u32x4*)(x + grow * DM + i * 256 + (lane & 31) * 8);
      gw[i] = *(const u32x4*)(gatew + i * 256 + (lane & 31) * 8);
    }
#pragma unroll
    for (int mt = 0; mt < 4; ++mt) ST16(pack8(acc[mt][0], acc[mt][1]), WS_QF + (c * 128 + m16 + mt) * 1024);
    const int j0 = 7 * c + ((c & 1) ? 3 : 0);
    const unsigned hp = WS_AP + ((j0 >> 1) * 128 + m16) * 1024;
#pragma unroll
    for (int mt = 0; mt < 4; ++mt) {
      const f32x4 h0 = geglu4(acc[mt][2], acc[mt][6]), h1 = geglu4(acc[mt][3], acc[mt][7]);
      ST16(pack8(h0, h1), hp + mt * 1024);
      const f32x4 h2 = geglu4(acc[mt][4], acc[mt][8]), h3 = geglu4(acc[mt][5], acc[mt][9]);
      ST16(pack8(h2, h3), hp + 131072 + mt * 1024);
    }
    {
      float s = 0.f;
#pragma unroll
      for (int i = 0; i < 4; ++i)
#pragma unroll
        for (int e = 0; e < 4; ++e) {
          const unsigned pr = pk2(__uint_as_float(gx[i][e] << 16) * __uint_as_float(gw[i][e] << 16), __uint_as_float(gx[i][e] & 0xffff0000u) * __uint_as_float(gw[i][e] & 0xffff0000u));
          s += __uint_as_float(pr << 16) + __uint_as_float(pr & 0xffff0000u);
        }
#pragma unroll
      for (int o = 1; o < 32; o <<= 1) s += __shfl_xor(s, o);
      s = bfr(s);
      const float gv = bfr(1.0f / (1.0f + __expf(-s)));
      if ((lane & 31) == 0) ((float*)(ws + WS_G))[grow] = gv;
      if (wm == 0 && lane == 0 && b < 136) ((unsigned*)(ws + WS_FLAGS))[b * 32] = 0u;
    }
  }
  LGKM_BAR();
  {
    int tid = w * 64 + __builtin_amdgcn_mbcnt_hi(~0u, __builtin_amdgcn_mbcnt_lo(~0u, 0u)); asm volatile("" : "+v"(tid));
    const unsigned rd = lb + 65536 + tid * 16;
    const f32x4 s = *(LAS f32x4*)(rd) + *(LAS f32x4*)(rd + 8192) + *(LAS f32x4*)(rd + 16384) + *(LAS f32x4*)(rd + 24576);
    *(f32x4*)(ws + WS_KVP + ((size_t)(c * 8 + rb) * 512 + tid) * 16) = s;
    if (tid < 16) {
      const unsigned rz = lb + 65536 + 32768 + tid * 16;
      const f32x4 zs = *(LAS f32x4*)(rz) + *(LAS f32x4*)(rz + 256) + *(LAS f32x4*)(rz + 512) + *(LAS f32x4*)(rz + 768);
      *(f32x4*)(ws + WS_ZP + ((size_t)(c * 8 + rb) * 16 + tid) * 16) = zs;
    }
  }
}

constexpr unsigned K2_SLOT = 32768, K2_KV = 131072, K2_LDS = 131072 + 4608;
constexpr int K2_NPIECE = 10;
constexpr int S_OWN = 20, S_ALL = 36, S_O = 28;
#define VM_BAR(N) do { asm volatile("" ::: "memory"); __builtin_amdgcn_s_waitcnt(((N) & 15) | 0xF70 | (((N) >> 4) << 14)); __builtin_amdgcn_s_barrier(); asm volatile("" ::: "memory"); } while (0)
#define BAR() do { asm volatile("" ::: "memory"); __builtin_amdgcn_s_barrier(); asm volatile("" ::: "memory"); } while (0)

DI void wait_ge(const unsigned* flag, unsigned need) {
  for (unsigned spin = 0; spin < (1u << 22); ++spin) {
    if (__hip_atomic_load((const GAS unsigned*)flag, __ATOMIC_RELAXED, __HIP_MEMORY_SCOPE_AGENT) >= need) break;
    __builtin_amdgcn_s_sleep(2);
  }
  asm volatile("s_waitcnt vmcnt(0)" ::: "memory");
}
#define lda(d, srd, voff, soff) d = __builtin_bit_cast(bf16x8, __builtin_amdgcn_raw_buffer_load_b128(srd, voff, soff, 16))

template <bool helper>
DI void k2_compute(char* __restrict__ ws, uint16_t* __restrict__ out, const unsigned lb, const int w, const int mt128, const int nt128, const int oc) {
  ST(unsigned long long wsum = 0, r0 = rt_(), t0 = mt_();)
  const int lane = threadIdx.x & 63;
  const int g = lane >> 4, q = (lane & 15) >> 2, p = lane & 3;
  const int pair = mt128 * 8 + nt128;
  constexpr int S0 = helper ? S_OWN : 0, S1 = helper ? S_ALL : S_OWN;
  const int kg = w >> 2, wm = w & 3;
  const float* const gs = (const float*)(ws + WS_G);
  const auto sa = __builtin_amdgcn_make_buffer_rsrc((void*)(ws + WS_AP), 0, 144u * 131072u, 0x00020000);
  const unsigned va = lane * 16;
  unsigned ao = (4 * S0 + 2 * kg) * 131072 + (mt128 * 8 + 2 * wm) * 1024;
  bf16x8 ar[4][2];
  lda(ar[0][0], sa, va, ao); lda(ar[0][1], sa, va, ao + 1024);
  lda(ar[1][0], sa, va, ao + 131072); lda(ar[1][1], sa, va, ao + 131072 + 1024);
  lda(ar[2][0], sa, va, ao + 524288); lda(ar[2][1], sa, va, ao + 524288 + 1024);
  ao += 524288;
  float gsc[2];
  gsc[0] = gs[mt128 * 128 + wm * 32 + (helper ? 0 : kg * 16) + (lane & 15)];
  gsc[1] = helper ? gs[mt128 * 128 + wm * 32 + 16 + (lane & 15)] : 0.f;
  {
    const char* kp = ws + WS_KVP + ((size_t)oc * 8 * 512 + w * 64 + lane) * 16;
    f32x4 kvl[8];
#pragma unroll
    for (int r = 0; r < 8; ++r) kvl[r] = *(const f32x4*)(kp + r * 8192);
    const char* zp = ws + WS_ZP + ((size_t)oc * 8 * 16 + 2 * g * 16 + (lane & 15)) * 16;
    f32x4 zs = *(const f32x4*)zp + *(const f32x4*)(zp + 256);
    const f32x4 kvs = ((kvl[0] + kvl[1]) + (kvl[2] + kvl[3])) + ((kvl[4] + kvl[5]) + (kvl[6] + kvl[7]));
    *(LAS u32x2*)(lb + K2_KV + ((w & 1) * 2 + (w >> 2)) * 1024 + lane * 16 + ((w >> 1) & 1) * 8) = (u32x2){pk2(kvs[0], kvs[1]), pk2(kvs[2], kvs[3])};
    float z0 = zs[0], z1 = zs[1], z2 = zs[2], z3 = zs[3]; XRED2(z0, z1, fadd_); XRED2(z2, z3, fadd_);
    if (w == 0) *(LAS u32x2*)(lb + K2_KV + 4096 + ((lane >> 3) & 1) * 64 + (lane & 3) * 16 + ((lane >> 2) & 1) * 8) = (u32x2){pk2(z0, z1), pk2(z2, z3)};
  }
  f32x4 acc[2][8];
#pragma unroll
  for (int m = 0; m < 2; ++m)
#pragma unroll
    for (int n = 0; n < 8; ++n) acc[m][n] = (f32x4){0.f, 0.f, 0.f, 0.f};
  const unsigned bb0 = lb + (64 * kg + 4 * g + q) * 256 + (((4 * (g & 1) + q) << 1 | (p >> 1)) << 4) + (p & 1) * 8;
  unsigned bs = bb0;
  unsigned sl = 0;
  ST(int stepi = 0;)
  bf16x8 bw[8];
  auto ldB = [&](int u) -> bf16x8 { const unsigned a = (bs ^ ((u & 7) << 5)) + (u >> 3) * 8192; return trrd2(a, a + 4096); };
  __builtin_amdgcn_s_waitcnt(0xC07F);
  BAR();
  ST(unsigned long long t1 = mt_();)
#pragma unroll
  for (int u = 0; u < 6; ++u) bw[u] = ldB(u);
  auto step = [&](auto par_, auto ia0_, auto ia1_, auto nxt_) {
    constexpr int par = decltype(par_)::value;
    constexpr bool ia0 = decltype(ia0_)::value, ia1 = decltype(ia1_)::value, nxt = decltype(nxt_)::value;
#pragma unroll
    for (int u = 0; u < 16; ++u) {
      if (u == 10 && nxt) {
        BAR();
        sl = (sl + K2_SLOT) & (3 * K2_SLOT);
        bs = bb0 + sl; asm volatile("" : "+v"(bs));
      }
      if (u < 10 || nxt) bw[(u + 6) & 7] = ldB((u + 6) & 15);
      if (ia0 && u < 2) lda(ar[(2 * par + 3) & 3][u], sa, va, ao + 131072 + u * 1024);
      if (ia1 && (u == 8 || u == 9)) lda(ar[2 * par][u - 8], sa, va, ao + 524288 + (u - 8) * 1024);
#pragma unroll
      for (int m = 0; m < 2; ++m) acc[m][u & 7] = mfma16(bw[u & 7], ar[2 * par + (u >> 3)][m], acc[m][u & 7]);
      SB();
    }
    ao += 524288;
  };
  const auto rp = __builtin_amdgcn_make_buffer_rsrc((void*)(ws + WS_PART), 0, 128u * 65536u, 0x00020000);
  const unsigned po = pair * 65536 + w * 8192;
  u32x4 part[8];
  if (helper) {
#pragma unroll 1
    for (int S = S0; S < S_O; S += 2) { step(I0{}, I1{}, I1{}, I1{}); step(I1{}, I1{}, I1{}, I1{}); }
#pragma unroll
    for (int m = 0; m < 2; ++m) {
      const float sc = bfr(1.0f - gsc[m]);
#pragma unroll
      for (int n = 0; n < 8; ++n) acc[m][n] *= sc;
    }
#pragma unroll 1
    for (int S = S_O; S < S1 - 2; S += 2) { step(I0{}, I1{}, I1{}, I1{}); step(I1{}, I1{}, I1{}, I1{}); }
    ST(wsum = mt_();)
    step(I0{}, I1{}, I0{}, I1{});
    step(I1{}, I0{}, I0{}, I0{});
  } else {
#pragma unroll 1
    for (int S = S0; S < S1 - 2; S += 2) { step(I0{}, I1{}, I1{}, I1{}); step(I1{}, I1{}, I1{}, I1{}); }
    ST(wsum = mt_();)
    step(I0{}, I1{}, I0{}, I1{});
#pragma unroll
    for (int i = 0; i < 8; ++i) part[i] = __builtin_amdgcn_raw_buffer_load_b128(rp, va, po + i * 1024, 16);
    step(I1{}, I0{}, I0{}, I0{});
  }
  ST(unsigned long long t2 = mt_();)
  f32x4 fin[8];
  const unsigned ex = lb + ((sl + K2_SLOT + kg * K2_SLOT) & (3 * K2_SLOT)) + wm * 8192 + va, exp_ = lb + ((sl + 2 * K2_SLOT - kg * K2_SLOT) & (3 * K2_SLOT)) + wm * 8192 + va;
  if (kg == 0) {
#pragma unroll
    for (int n = 0; n < 8; ++n) *(LAS f32x4*)(exp_ + n * 1024) = acc[1][n];
    LGKM_BAR();
#pragma unroll
    for (int n = 0; n < 8; ++n) fin[n] = acc[0][n] + *(LAS f32x4*)(ex + n * 1024);
  } else {
#pragma unroll
    for (int n = 0; n < 8; ++n) *(LAS f32x4*)(exp_ + n * 1024) = acc[0][n];
    LGKM_BAR();
#pragma unroll
    for (int n = 0; n < 8; ++n) fin[n] = acc[1][n] + *(LAS f32x4*)(ex + n * 1024);
  }
  if (helper) {
#pragma unroll
    for (int n = 0; n < 8; ++n) __builtin_amdgcn_raw_buffer_store_b128(__builtin_bit_cast(u32x4, fin[n]), rp, va + po + n * 1024, 0, 16);
    WAIT_BAR(0);
    if (threadIdx.x == 0) __hip_atomic_store((GAS unsigned*)((unsigned*)(ws + WS_FLAGS) + (8 + pair) * 32), 1u, __ATOMIC_RELAXED, __HIP_MEMORY_SCOPE_AGENT);
  } else {
    const float omg = bfr(1.0f - gsc[0]);
    uint16_t* orow = out + (size_t)(mt128 * 128 + wm * 32 + kg * 16 + (lane & 15)) * DM + nt128 * 128 + 4 * g;
#pragma unroll
    for (int n = 0; n < 8; ++n) {
      const f32x4 v = fin[n] * omg + __builtin_bit_cast(f32x4, part[n]);
      *(u32x2*)(orow + n * 16) = (u32x2){pk2(v[0], v[1]), pk2(v[2], v[3])};
    }
  }
}

__global__ __launch_bounds__(768, 3) void k2_kernel(const uint16_t* __restrict__ wdown, const uint16_t* __restrict__ wo, char* __restrict__ ws,
                                                    uint16_t* __restrict__ out) {
  extern __shared__ __attribute__((aligned(16))) char smem[];
  const unsigned lb = (unsigned)(size_t)(LAS char*)smem;
  const int w = __builtin_amdgcn_readfirstlane(threadIdx.x >> 6);
  const int b = blockIdx.x, xj = b & 7, bi = b >> 3;
  const int helper = xj >> 2;
  const int mt128 = 4 * (xj & 3) + (bi >> 3), nt128 = bi & 7;
  const int oc = 4 * xj + (bi & 3), orb = bi >> 2;
  if (w < 8) {
    if (helper) k2_compute<true>(ws, out, lb, w, mt128, nt128, oc);
    else k2_compute<false>(ws, out, lb, w, mt128, nt128, oc);
    return;
  }
  const int sw = w - 8, lane = threadIdx.x & 63, g = lane >> 4;
  const int S0 = helper ? S_OWN : 0, S1 = helper ? S_ALL : S_OWN;
  unsigned* const flags = (unsigned*)(ws + WS_FLAGS);
  const float* const gs = (const float*)(ws + WS_G);
  const u32x4 sd = mk_srd(wdown, DF * DM * 2), so_ = mk_srd(wo, DM * DM * 2);
  const unsigned vb0 = (32 * sw + g) * 2048 + nt128 * 256 + (((lane & 15) ^ (g << 1)) << 4), vb1 = (vb0 ^ 128) + 4 * 2048;
  auto stage = [&](int S) {
    if (S < S1) {
      u32x4 r;
#pragma unroll
      for (int j = 0; j < 4; ++j) r[j] = S >= S_O ? so_[j] : sd[j];
      const unsigned so = (S >= S_O ? S - S_O : S) * 262144, dst = lb + (S & 3) * K2_SLOT + sw * 8192;
#pragma unroll
      for (int i = 0; i < 8; ++i) dma16(r, dst + i * 1024, (i & 1) ? vb1 : vb0, so + (i >> 1) * 16384);
    }
  };
  stage(S0); stage(S0 + 1);
  bf16x8 qf[4]; float go[4];
  const bf16x8 wp0 = *(const bf16x8*)(ws + WS_WPF + oc * 2048 + lane * 16), wp1 = *(const bf16x8*)(ws + WS_WPF + oc * 2048 + 1024 + lane * 16);
#pragma unroll
  for (int i = 0; i < 4; ++i) {
    qf[i] = *(const bf16x8*)(ws + WS_QF + ((size_t)oc * 128 + orb * 16 + 4 * sw + i) * 1024 + lane * 16);
    go[i] = gs[(orb * 16 + 4 * sw + i) * 16 + (lane & 15)];
  }
  bf16x8 pf0, pf1;
  auto fm = [&](int i) { f32x4 pp0, pp1, pn0, pn1; featmap(qf[i], wp0, wp1, pp0, pp1, pn0, pn1); pf0 = pack8(pp0, pp1); pf1 = pack8(pn0, pn1); };
  VM_BAR(K2_NPIECE + 8);
  stage(S0 + 2);
  bf16x8 ka[2][2], za[2];
#pragma unroll
  for (int ks = 0; ks < 2; ++ks) {
    ka[0][ks] = ldsrd(lb + K2_KV + (0 + ks) * 1024 + lane * 16);
    ka[1][ks] = ldsrd(lb + K2_KV + (2 + ks) * 1024 + lane * 16);
    za[ks] = ldsrd(lb + K2_KV + 4096 + ks * 64 + g * 16);
  }
  const auto rs = __builtin_amdgcn_make_buffer_rsrc((void*)(ws + WS_AP), 0, 144u * 131072u, 0x00020000);
  auto fin = [&](int i) {
    const f32x4 z4 = {0.f, 0.f, 0.f, 0.f};
    f32x4 n0 = mfma16(ka[0][1], pf1, mfma16(ka[0][0], pf0, z4));
    f32x4 n1 = mfma16(ka[1][1], pf1, mfma16(ka[1][0], pf0, z4));
    f32x4 dn = mfma16(za[1], pf1, mfma16(za[0], pf0, z4));
    const float sc = go[i] * __builtin_amdgcn_rcpf(dn[0] + 1e-6f);
    n0 *= sc; n1 *= sc;
    __builtin_amdgcn_raw_buffer_store_b128(__builtin_bit_cast(u32x4, pack8(n0, n1)), rs, lane * 16 + ((112 + oc) * 128 + orb * 16 + 4 * sw + i) * 1024, 0, 16  );
    asm volatile("" ::: "memory");
  };
  fm(0); fin(0);
  SB();
  VM_BAR(8);
  stage(S0 + 3);
  fm(1); fin(1);
  SB();
  VM_BAR(8);
  stage(S0 + 4);
  fm(2); fin(2);
  SB();
  VM_BAR(8);
  fm(3); fin(3);
  stage(S0 + 5);
  VM_BAR(8);
  if (threadIdx.x == 512) __hip_atomic_fetch_add((GAS unsigned*)(flags + orb * 32), 1u, __ATOMIC_RELAXED, __HIP_MEMORY_SCOPE_AGENT);
  for (int S = S0 + 4; S < S1 - 1; ++S) {
    stage(S + 2);
    if (sw == 3) {
      if (S == S_O - 3 && lane == 0) wait_ge(flags + (mt128 >> 1) * 32, 32u);
      if (S == S1 - 2 && !helper && lane == 0) wait_ge(flags + (8 + mt128 * 8 + nt128) * 32, 1u);
    }
    if (S + 2 < S1) VM_BAR(8); else VM_BAR(0);
  }
}

extern "C" void kernel_launch(void* const* d_in, const int* in_sizes, int n_in, void* d_out, int out_size, void* d_ws, size_t ws_size,
                              hipStream_t stream) {
  const uint16_t* x = (const uint16_t*)d_in[0];
  const uint16_t* wqkv = (const uint16_t*)d_in[1];
  const uint16_t* wphi = (const uint16_t*)d_in[2];
  const uint16_t* wo = (const uint16_t*)d_in[3];
  const uint16_t* wg = (const uint16_t*)d_in[5];
  const uint16_t* wu = (const uint16_t*)d_in[6];
  const uint16_t* wd = (const uint16_t*)d_in[7];
  const uint16_t* gw = (const uint16_t*)d_in[8];
  (void)hipFuncSetAttribute((const void*)k1_kernel, hipFuncAttributeMaxDynamicSharedMemorySize, K1_LDS);
  (void)hipFuncSetAttribute((const void*)k2_kernel, hipFuncAttributeMaxDynamicSharedMemorySize, K2_LDS);
  k1_kernel<<<256, 512, K1_LDS, stream>>>(x, wqkv, wphi, wg, wu, gw, wd, wo, (char*)d_ws);
  k2_kernel<<<256, 768, K2_LDS, stream>>>(wd, wo, (char*)d_ws, (uint16_t*)d_out);
}
```

```cpp
#include <hip/hip_runtime.h>
#include <hip/hip_bf16.h>
#include <stdint.h>
#include <type_traits>
#pragma clang diagnostic ignored "-Wint-to-pointer-cast"

typedef short bf16x8 __attribute__((ext_vector_type(8)));
typedef short s16x4 __attribute__((ext_vector_type(4)));
typedef float f32x4 __attribute__((ext_vector_type(4)));
typedef float f32x2 __attribute__((ext_vector_type(2)));
typedef unsigned u32x4 __attribute__((ext_vector_type(4)));
typedef unsigned u32x2 __attribute__((ext_vector_type(2)));
typedef __bf16 bf16x2_t __attribute__((ext_vector_type(2)));
#define LAS __attribute__((address_space(3)))
#define GAS __attribute__((address_space(1)))
#define DI __device__ __forceinline__

constexpr int SEQ = 2048, DM = 1024, DF = 3584;
constexpr size_t WS_FLAGS = 0;
constexpr size_t WS_G     = 32768;
constexpr size_t WS_ZP    = 65536;
constexpr size_t WS_KVP   = 262144;
constexpr size_t WS_WPF   = 3u << 20;
constexpr size_t WS_QF    = 4u << 20;
constexpr size_t WS_AP    = 16u << 20;
constexpr size_t WS_PART  = 40u << 20;

#define ABL 0
DI f32x4 mfma16(bf16x8 a, bf16x8 b, f32x4 c) { return __builtin_amdgcn_mfma_f32_16x16x32_bf16(a, b, c, 0, 0, 0); }
DI void mfma_ip(f32x4& c, bf16x8 a, bf16x8 b) { asm volatile("v_mfma_f32_16x16x32_bf16 %0, %1, %2, %0" : "+v"(c) : "v"(a), "v"(b)); }
#define MFMA_FENCE() asm volatile("s_nop 7\n\ts_nop 7" ::: "memory")
DI s16x4 trrd(unsigned a) { return __builtin_amdgcn_ds_read_tr16_b64_v4i16((LAS s16x4*)a); }
DI bf16x8 trrd2(unsigned a0, unsigned a1) { s16x4 lo = trrd(a0), hi = trrd(a1); return __builtin_shufflevector(lo, hi, 0, 1, 2, 3, 4, 5, 6, 7); }
DI bf16x8 ldsrd(unsigned a) { return *(LAS bf16x8*)a; }
DI unsigned pk2(float lo, float hi) { f32x2 v = {lo, hi}; bf16x2_t b = __builtin_convertvector(v, bf16x2_t); return __builtin_bit_cast(unsigned, b); }
DI bf16x8 pack8(f32x4 a, f32x4 b) { u32x4 r = {pk2(a[0], a[1]), pk2(a[2], a[3]), pk2(b[0], b[1]), pk2(b[2], b[3])}; return __builtin_bit_cast(bf16x8, r); }
DI f32x4 unpk_lo(u32x4 v) { return (f32x4){__uint_as_float(v[0] << 16), __uint_as_float(v[0] & 0xffff0000u), __uint_as_float(v[1] << 16), __uint_as_float(v[1] & 0xffff0000u)}; }
DI f32x4 unpk_hi(u32x4 v) { return (f32x4){__uint_as_float(v[2] << 16), __uint_as_float(v[2] & 0xffff0000u), __uint_as_float(v[3] << 16), __uint_as_float(v[3] & 0xffff0000u)}; }
DI float bfr(float x) { unsigned u = pk2(x, x); return __uint_as_float(u << 16); }
DI u32x4 mk_srd(const void* p, unsigned bytes) {
  uint64_t a = (uint64_t)p;
  u32x4 r;
  r[0] = __builtin_amdgcn_readfirstlane((unsigned)a);
  r[1] = __builtin_amdgcn_readfirstlane((unsigned)(a >> 32) & 0xffffu);
  r[2] = __builtin_amdgcn_readfirstlane(bytes);
  r[3] = 0x00020000u;
  return r;
}
DI void pfetch(u32x4 srd, unsigned lds, unsigned voff, unsigned soff) {
  asm volatile("s_mov_b32 m0, %0\n\ts_nop 0\n\tbuffer_load_dword %1, %2, %3 offen lds" ::"s"(lds), "v"(voff), "s"(srd), "s"(soff) : "memory");
}
DI void dma16(u32x4 srd, unsigned lds, unsigned voff, unsigned soff) {
  asm volatile("s_mov_b32 m0, %0\n\ts_nop 0\n\tbuffer_load_dwordx4 %1, %2, %3 offen lds" ::"s"(lds), "v"(voff), "s"(srd), "s"(soff) : "memory");
}
#define WAIT_BAR(N) do { asm volatile("" ::: "memory"); __builtin_amdgcn_s_waitcnt(((N) & 15) | 0x70 | (((N) >> 4) << 14)); __builtin_amdgcn_s_barrier(); asm volatile("" ::: "memory"); } while (0)
#define LGKM_BAR() do { asm volatile("" ::: "memory"); __builtin_amdgcn_s_waitcnt(0xC07F); __builtin_amdgcn_s_barrier(); asm volatile("" ::: "memory"); } while (0)
#define SB() __builtin_amdgcn_sched_barrier(0)
#define ST(...)
#define WAIT_BAR_T(N) WAIT_BAR(N)

DI float fmx(float a, float b) { return __builtin_amdgcn_fmed3f(a, b, __builtin_inff()); }
#define XRED2(a, b, OP) do { \
    auto r1_ = __builtin_amdgcn_permlane16_swap(__float_as_uint(a), __float_as_uint(b), false, false); \
    float m_ = OP(__uint_as_float(r1_[0]), __uint_as_float(r1_[1])); \
    auto r2_ = __builtin_amdgcn_permlane32_swap(__float_as_uint(m_), __float_as_uint(m_), false, false); \
    m_ = OP(__uint_as_float(r2_[0]), __uint_as_float(r2_[1])); \
    auto r3_ = __builtin_amdgcn_permlane16_swap(__float_as_uint(m_), __float_as_uint(m_), false, false); \
    a = __uint_as_float(r3_[0]); b = __uint_as_float(r3_[1]); } while (0)
DI float fadd_(float a, float b) { return a + b; }
DI f32x4 exp2v(f32x4 v) { return (f32x4){__builtin_amdgcn_exp2f(v[0]), __builtin_amdgcn_exp2f(v[1]), __builtin_amdgcn_exp2f(v[2]), __builtin_amdgcn_exp2f(v[3])}; }

DI void featmap(bf16x8 fr, bf16x8 wp0, bf16x8 wp1, f32x4& pp0, f32x4& pp1, f32x4& pn0, f32x4& pn1) {
  const f32x4 z4 = {0.f, 0.f, 0.f, 0.f};
  constexpr float L2E = 1.4426950408889634f;
  const f32x4 t0 = mfma16(wp0, fr, z4) * L2E, t1 = mfma16(wp1, fr, z4) * L2E;
  float mx = fmaxf(fmaxf(fmaxf(t0[0], t0[1]), fmaxf(t0[2], t0[3])), fmaxf(fmaxf(t1[0], t1[1]), fmaxf(t1[2], t1[3])));
  float nm = -fminf(fminf(fminf(t0[0], t0[1]), fminf(t0[2], t0[3])), fminf(fminf(t1[0], t1[1]), fminf(t1[2], t1[3])));
  XRED2(mx, nm, fmx);
  pp0 = exp2v(t0 - mx); pp1 = exp2v(t1 - mx);
  pn0 = exp2v(-nm - t0); pn1 = exp2v(-nm - t1);
  const f32x4 s4p = pp0 + pp1, s4n = pn0 + pn1;
  float sp = (s4p[0] + s4p[1]) + (s4p[2] + s4p[3]), sn = (s4n[0] + s4n[1]) + (s4n[2] + s4n[3]);
  XRED2(sp, sn, fadd_);
  const float ip = __builtin_amdgcn_rcpf(sp), in = __builtin_amdgcn_rcpf(sn);
  pp0 *= ip; pp1 *= ip; pn0 *= in; pn1 *= in;
}

DI f32x4 geglu4(f32x4 g, f32x4 u) {
  constexpr float C1 = -2.0f * 0.7978845608028654f * 1.4426950408889634f, C2 = C1 * 0.044715f;
  const f32x4 d = exp2v(g * (g * g * C2 + C1)) + 1.0f;
  const f32x4 r = {__builtin_amdgcn_rcpf(d[0]), __builtin_amdgcn_rcpf(d[1]), __builtin_amdgcn_rcpf(d[2]), __builtin_amdgcn_rcpf(d[3])};
  return (g * u) * r;
}
#define ST_AUX 16
DI void prio_if(int hi) { asm volatile("s_setprio 0\n\ts_cmp_eq_u32 %0, 0\n\ts_cbranch_scc1 1f\n\ts_setprio 1\n1:" ::"s"(hi) : "scc"); }
#define PRIO2 0
#define PRIO1 0
using I0 = std::integral_constant<int, 0>; using I1 = std::integral_constant<int, 1>;

#define K1STG(x) x
constexpr unsigned K1_X0 = 0, K1_XS = K1STG(32768), K1_W0 = 65536, K1_WS = K1STG(40960), K1_WG = 12288, K1_WU = 26624, K1_PHI = 147456, K1_PF = 149504, K1_LDS = 151552;

struct K1Dma {
  u32x4 sa, sb, sc;
  unsigned co[4], cc[2];
  unsigned lo[10];
  unsigned stab, stc;
};

template <int WN>
DI void k1_body(unsigned long long& wsum, f32x4 (&acc)[4][10], const unsigned lb, const int wm, const int lane,
                const u32x4 sx, const unsigned v0, const unsigned v1, const unsigned v2, const unsigned vxa, const unsigned vxb, const K1Dma& D) {
  const int g = lane >> 4, q = (lane & 15) >> 2, p = lane & 3, fr = lane & 15;
  const unsigned xrow = wm * 64 + fr;
  const unsigned xb0 = lb + K1_X0 + xrow * 128 + ((g ^ ((xrow >> 1) & 7)) << 4);
  const unsigned xb1 = xb0 ^ 64;
  const unsigned qb0 = lb + K1_W0 + (8 * g + q) * 192 + ((0 ^ (g & 1)) << 5) + p * 8;
  const unsigned qb1 = lb + K1_W0 + (8 * g + q) * 192 + ((1 ^ (g & 1)) << 5) + p * 8;
  const unsigned gb0 = lb + K1_W0 + K1_WG + (8 * g + 4 * (0 ^ (g & 1)) + q) * 224 + p * 8;
  const unsigned gb1 = lb + K1_W0 + K1_WG + (8 * g + 4 * (1 ^ (g & 1)) + q) * 224 + p * 8;
  const unsigned xl = lb + K1_X0 + wm * 8192, wl = lb + K1_W0;
  unsigned sxo = 128, tab = D.stab, tc = D.stc;
  int pt = 3;

  bf16x8 xf[2][4], wf[4];
  auto ldW = [&](int st, int u) -> bf16x8 {
    const int ks = u / 10, i = u % 10;
    const unsigned so = st * K1_WS;
    constexpr int NQ = WN == 0 ? 4 : 2;
    constexpr int NG = WN == 0 ? 3 : 4;
    constexpr int P0 = WN == 0 ? 4 : 0;
    if (i < NQ) {
      const int ptl = WN == 0 ? 1 + (i >> 1) : 0, nh = i & 1;
      const unsigned b = (nh ? qb1 : qb0) + so + ks * 32 * 192 + ptl * 64;
      return trrd2(b, b + 4 * 192);
    } else {
      const int j = i - NQ, up = j / NG, P = P0 + j % NG;
      const unsigned o = so + ks * 32 * 224 + up * (K1_WU - K1_WG) + P * 32;
      return trrd2(gb0 + o, gb1 + o);
    }
  };
  auto ldX = [&](int st, int ks, int mt) -> bf16x8 { return ldsrd((ks ? xb1 : xb0) + st * K1_XS + mt * 2048); };
  auto mma = [&](int u) {
    const int ks = u / 10, i = u % 10;
#pragma unroll
    for (int mt = 0; mt < 4; ++mt) {
      if (WN == 0 && (i == 2 || i == 3)) acc[mt][i] = mfma16(xf[ks][mt], wf[u & 3], acc[mt][i]);
      else acc[mt][i] = mfma16(wf[u & 3], xf[ks][mt], acc[mt][i]);
    }
  };
  auto dmaX = [&](int st, int j) { if (ABL != 10) dma16(sx, xl + st * K1_XS + j * 1024, (j & 1) ? vxb : vxa, sxo + j * 16384); };
  auto dmaW = [&](int st, int s) {
    if (ABL == 10) return;
    if (s < 8) dma16((s & 2) ? D.sb : D.sa, wl + st * K1_WS + D.lo[s], s < 4 ? v0 : v1, tab + D.co[s & 3]);
    else dma16(D.sc, wl + st * K1_WS + D.lo[s], v2, tc + D.cc[s - 8]);
  };
  auto warm = [&](int k) {   if (ABL == 10) return;
    int i = pt - (int)D.co[2 * k]; i = i < 0 ? 0 : i; i = i > (int)D.co[2 * k + 1] ? (int)D.co[2 * k + 1] : i;
    pfetch(k ? D.sb : D.sa, lb + K1_PF + wm * 512 + k * 256, k ? v1 : v0, (unsigned)i * (k ? D.stc : D.stab));
  };

#pragma unroll
  for (int mt = 0; mt < 4; ++mt) xf[0][mt] = ldX(0, 0, mt);
  wf[0] = ldW(0, 0); wf[1] = ldW(0, 1); wf[2] = ldW(0, 2);

  auto step = [&](auto st_, auto dox_, auto dow_) {
    constexpr int st = decltype(st_)::value;
    constexpr bool dox = decltype(dox_)::value, dow = decltype(dow_)::value;
#pragma unroll
    for (int u = 0; u < 17; ++u) {
      if (PRIO1 && ((u + 2) % (PRIO1 ? PRIO1 : 1)) == 0) { if ((((u + 2) / (PRIO1 ? PRIO1 : 1)) & 1) ^ WN) __builtin_amdgcn_s_setprio(1); else __builtin_amdgcn_s_setprio(0); }
      wf[(u + 3) & 3] = ldW(st, u + 3);
      if (u >= 2 && u < 6) xf[1][u - 2] = ldX(st, 1, u - 2);
      if (WN == 0 && dox) {
        if (u < 3) { dmaW(st ^ 1, 4 + 2 * u); dmaW(st ^ 1, 5 + 2 * u); }
        else if (u < 7) { dmaX(st ^ 1, 2 * (u - 3)); dmaX(st ^ 1, 2 * (u - 3) + 1); }
      }
      if (WN == 1 && dow && (u == 4 || u == 8)) warm(u == 8);
      mma(u);
      SB();
    }
    if (WN == 0) { sxo += 128; tab += D.stab; tc += D.stc; } else ++pt;
    mma(17);
    SB();
    if (WN == 0) WAIT_BAR_T(0); else WAIT_BAR_T(63);
    if (dox) {
#pragma unroll
      for (int mt = 0; mt < 4; ++mt) xf[0][mt] = ldX(st ^ 1, 0, mt);
      wf[0] = ldW(st ^ 1, 0); wf[1] = ldW(st ^ 1, 1);
    }
    if (PRIO1) __builtin_amdgcn_s_setprio(WN);
    if (WN == 0 && dow) { dmaW(st, 0); dmaW(st, 1); }
    mma(18);
    SB();
    if (dox) wf[2] = ldW(st ^ 1, 2);
    if (WN == 0 && dow) { dmaW(st, 2); dmaW(st, 3); }
    mma(19);
    SB();
  };
  for (int t2 = 0; t2 < 7; ++t2) { step(I0{}, I1{}, I1{}); step(I1{}, I1{}, I1{}); }
  step(I0{}, I1{}, I0{});
  step(I1{}, I0{}, I0{});
}

__global__ __launch_bounds__(512, 2) void k1_kernel(const uint16_t* __restrict__ x, const uint16_t* __restrict__ wqkv, const uint16_t* __restrict__ wphi,
                                                    const uint16_t* __restrict__ wg, const uint16_t* __restrict__ wu, const uint16_t* __restrict__ gatew,
                                                    const uint16_t* __restrict__ wdown, const uint16_t* __restrict__ wo, char* __restrict__ ws) {
  extern __shared__ __attribute__((aligned(16))) char smem[];
  const unsigned lb = (unsigned)(size_t)(LAS char*)smem;
  unsigned long long wsum = 0; ST(unsigned long long r0 = rt_(); unsigned long long t0 = mt_(); unsigned long long t2 = 0;)
  const int tid = threadIdx.x, lane = tid & 63;
  const int w = __builtin_amdgcn_readfirstlane(tid >> 6);
  const int wn = w >> 2, wm = w & 3;
  const int b = blockIdx.x, xj = b & 7, bi = b >> 3;
  const int c = 4 * xj + (bi & 3), rb = bi >> 2;
  const u32x4 sx = mk_srd(x, SEQ * DM * 2);
  const u32x4 sq = mk_srd(wqkv, DM * 3072 * 2), sg = mk_srd(wg, DM * DF * 2), su = mk_srd(wu, DM * DF * 2);
  f32x4 acc[4][10];
#pragma unroll
  for (int m = 0; m < 4; ++m)
#pragma unroll
    for (int i = 0; i < 10; ++i) acc[m][i] = (f32x4){0.f, 0.f, 0.f, 0.f};
  K1Dma D;
  const int m16 = rb * 16 + wm * 4;
  const auto rw = __builtin_amdgcn_make_buffer_rsrc((void*)ws, 0, 64u << 20, 0x00020000);
#define EPI_LANE() int lane = __builtin_amdgcn_mbcnt_hi(~0u, __builtin_amdgcn_mbcnt_lo(~0u, 0u)); asm volatile("" : "+v"(lane)); const int g = lane >> 4;
#define ST16(val, so) __builtin_amdgcn_raw_buffer_store_b128(__builtin_bit_cast(u32x4, val), rw, lane * 16 + (unsigned)(so), 0, ST_AUX)
  ST(unsigned long long t1 = 0;)
  if (wn == 0) {
    const unsigned vxa = (rb * 256 + 64 * wm + (lane >> 3)) * 2048 + (((lane & 7) ^ (lane >> 4)) << 4), vxb = vxa ^ 64;
    if (wm < 2) dma16(mk_srd(wphi, 32 * 32 * 32 * 2), lb + K1_PHI + wm * 1024, lane * 16, c * 2048 + wm * 1024);
#pragma unroll
    for (int j = 0; j < 8; ++j) dma16(sx, lb + K1_X0 + wm * 8192 + j * 1024, (j & 1) ? vxb : vxa, j * 16384);
    const unsigned gtbl = (c & 1) ? 0x0216543u : 0x6543210u;
    auto pat = [&](bool isq, int pi) -> unsigned {
      const int ci = 64 * pi + lane, r = (ci * (isq ? 5462 : 4682)) >> 16, cc = ci - (isq ? 12 : 14) * r;
      const int o8 = (r >> 3) & 1;
      const unsigned vq = r * 6144 + ((cc >> 2) * 1024 + 32 * c + (((cc >> 1) & 1) ^ o8) * 16 + (cc & 1) * 8) * 2;
      const unsigned vg = (r ^ (o8 << 2)) * 7168 + (112 * c + 16 * ((gtbl >> (4 * (cc >> 1))) & 7) + 8 * (cc & 1)) * 2;
      return isq ? vq : vg;
    };
    const bool q01 = wm == 3, q2 = wm >= 2;
    const unsigned v0 = pat(q01, q01 ? 0 : 2 * wm), v1 = pat(q01, q01 ? 1 : 2 * wm + 1), v2 = pat(q2, q2 ? 2 : 6);
#pragma unroll
    for (int i = 0; i < 4; ++i) { D.sa[i] = q01 ? sq[i] : sg[i]; D.sb[i] = q01 ? sq[i] : su[i]; D.sc[i] = q2 ? sq[i] : (wm ? su[i] : sg[i]); }
    D.stab = q01 ? 64 * 6144 : 64 * 7168; D.stc = q2 ? 64 * 6144 : 64 * 7168;
    D.co[0] = 0; D.co[1] = q01 ? 16 * 6144 : 32 * 7168; D.co[2] = q01 ? 32 * 6144 : 0; D.co[3] = q01 ? 48 * 6144 : 32 * 7168;
#pragma unroll
    for (int h = 0; h < 2; ++h)
#pragma unroll
      for (int i = 0; i < 4; ++i) D.lo[4 * h + i] = q01 ? h * 1024 + i * 3072 : K1_WG + (2 * wm + h) * 1024 + i * 7168;
    D.cc[0] = q2 ? (wm - 2) * 32 * 6144 : 0; D.cc[1] = D.cc[0] + (q2 ? 16 * 6144 : 32 * 7168);
    D.lo[8] = q2 ? 2048 + (wm - 2) * 6144 : K1_WG + wm * 14336 + 6144; D.lo[9] = D.lo[8] + (q2 ? 3072 : 7168);
#pragma unroll
    for (int st = 0; st < 2; ++st)
#pragma unroll
      for (int s = 0; s < (st ? (ABL == 10 ? 0 : 4) : 10); ++s) {
        if (s < 8) dma16((s & 2) ? D.sb : D.sa, lb + K1_W0 + st * K1_WS + D.lo[s], s < 4 ? v0 : v1, st * D.stab + D.co[s & 3]);
        else dma16(D.sc, lb + K1_W0 + st * K1_WS + D.lo[s], v2, st * D.stc + D.cc[s - 8]);
      }
    WAIT_BAR(ABL == 10 ? 0 : 4);
    ST(t1 = mt_();)
    k1_body<0>(wsum, acc, lb, wm, lane, sx, v0, v1, v2, vxa, vxb, D);
    ST(t2 = mt_();)
    EPI_LANE();
    const int q = (lane & 15) >> 2, p = lane & 3;
    const unsigned wpb = lb + K1_PHI + (4 * g + q) * 64 + p * 8;
    const bf16x8 wp0 = trrd2(wpb, wpb + 16 * 64), wp1 = trrd2(wpb + 32, wpb + 16 * 64 + 32);
    if (rb == 0 && wm == 0) { ST16(wp0, WS_WPF + c * 2048); ST16(wp1, WS_WPF + c * 2048 + 1024); }
    const unsigned pkb = lb + wm * 8704;
    const unsigned pw = pkb + (lane & 15) * 136 + g * 8;
#pragma unroll
    for (int mt = 0; mt < 4; ++mt) {
      f32x4 pp0, pp1, pn0, pn1;
      featmap(pack8(acc[mt][0], acc[mt][1]), wp0, wp1, pp0, pp1, pn0, pn1);
      *(LAS u32x2*)(pw + mt * 16 * 136 + 0)  = (u32x2){pk2(pp0[0], pp0[1]), pk2(pp0[2], pp0[3])};
      *(LAS u32x2*)(pw + mt * 16 * 136 + 32) = (u32x2){pk2(pp1[0], pp1[1]), pk2(pp1[2], pp1[3])};
      *(LAS u32x2*)(pw + mt * 16 * 136 + 64) = (u32x2){pk2(pn0[0], pn0[1]), pk2(pn0[2], pn0[3])};
      *(LAS u32x2*)(pw + mt * 16 * 136 + 96) = (u32x2){pk2(pn1[0], pn1[1]), pk2(pn1[2], pn1[3])};
    }
    asm volatile("s_waitcnt lgkmcnt(0)" ::: "memory");
    f32x4 kv[4][2], zz[4];
#pragma unroll
    for (int ft = 0; ft < 4; ++ft) { kv[ft][0] = (f32x4){0.f, 0.f, 0.f, 0.f}; kv[ft][1] = kv[ft][0]; zz[ft] = kv[ft][0]; }
    const bf16x8 ones = {0x3f80, 0x3f80, 0x3f80, 0x3f80, 0x3f80, 0x3f80, 0x3f80, 0x3f80};
    const unsigned pr = pkb + (4 * g + q) * 136 + p * 8;
#pragma unroll
    for (int mp = 0; mp < 2; ++mp) {
      const bf16x8 va = pack8(acc[2 * mp][2], acc[2 * mp + 1][2]), vb = pack8(acc[2 * mp][3], acc[2 * mp + 1][3]);
#pragma unroll
      for (int ft = 0; ft < 4; ++ft) {
        const bf16x8 a = trrd2(pr + (32 * mp) * 136 + ft * 32, pr + (32 * mp + 16) * 136 + ft * 32);
        kv[ft][0] = mfma16(a, va, kv[ft][0]);
        kv[ft][1] = mfma16(a, vb, kv[ft][1]);
        zz[ft] = mfma16(a, ones, zz[ft]);
      }
    }
    const unsigned rd = lb + 65536 + wm * 8192 + lane * 16;
#pragma unroll
    for (int ft = 0; ft < 4; ++ft) {
      *(LAS f32x4*)(rd + (ft * 2 + 0) * 1024) = kv[ft][0];
      *(LAS f32x4*)(rd + (ft * 2 + 1) * 1024) = kv[ft][1];
    }
    { const int ft = lane & 3;
      const f32x4 zsel = ft == 0 ? zz[0] : (ft == 1 ? zz[1] : (ft == 2 ? zz[2] : zz[3]));
      *(LAS f32x4*)(lb + 65536 + 32768 + wm * 256 + ft * 64 + g * 16) = zsel; }
    const int jp = 7 * c + ((c & 1) ? 1 : 4), js = 7 * c + ((c & 1) ? 0 : 6);
    const unsigned hp = WS_AP + ((jp >> 1) * 128 + m16) * 1024, hs = WS_AP + ((js >> 1) * 128 + m16) * 1024 + (js & 1) * 8;
#pragma unroll
    for (int mt = 0; mt < 4; ++mt) {
      const f32x4 h0 = geglu4(acc[mt][4], acc[mt][7]), h1 = geglu4(acc[mt][5], acc[mt][8]), h2 = geglu4(acc[mt][6], acc[mt][9]);
      ST16(pack8(h0, h1), hp + mt * 1024);
      __builtin_amdgcn_raw_buffer_store_b64((u32x2){pk2(h2[0], h2[1]), pk2(h2[2], h2[3])}, rw, lane * 16 + hs + mt * 1024, 0, ST_AUX);
    }
  } else {
    const u32x4 sdn = mk_srd(wdown, DF * DM * 2), swo = mk_srd(wo, DM * DM * 2);
    const int r8 = 8 * rb + ((lane >> 2) & 7), l3 = lane & 3;
    const unsigned vx_ = (rb * 256 + (c & 3) * 64 + 32 * wm + (lane >> 1)) * 2048 + (lane & 1) * 64;
    const unsigned vg_ = r8 * 7168 + ((224 * c) & ~63) + l3 * 64;
    const unsigned vq_ = r8 * 6144 + (l3 == 3 ? 0 : l3) * 2048 + 64 * c;
    const unsigned v0 = wm < 2 ? vx_ : (wm == 2 ? vg_ : vq_);
    const unsigned v1 = wm == 2 ? vg_ : (wm < 2 ? (b * 7 + 4 * wm) * 4096 : b * 2 * 4096) + lane * 64;
#pragma unroll
    for (int i = 0; i < 4; ++i) { D.sa[i] = wm < 2 ? sx[i] : (wm == 2 ? sg[i] : sq[i]); D.sb[i] = wm < 2 ? sdn[i] : (wm == 2 ? su[i] : swo[i]); }
    D.stab = wm < 2 ? 128 : (wm == 2 ? 64 * 7168 : 64 * 6144); D.stc = wm == 2 ? 64 * 7168 : 4096;
    D.co[0] = 0; D.co[1] = 15; D.co[2] = wm == 2 ? 0 : 7; D.co[3] = wm == 2 ? 15 : (wm == 3 ? 1 : 3 - wm);
#pragma unroll
    for (int t = 1; t < 3; ++t) {
      pfetch(D.sa, lb + K1_PF + wm * 512, v0, t * D.stab);
      if (wm == 2) pfetch(D.sb, lb + K1_PF + wm * 512 + 256, v1, t * D.stc);
    }
    WAIT_BAR(63);
    ST(t1 = mt_();)
    k1_body<1>(wsum, acc, lb, wm, lane, sx, v0, v1, 0u, 0u, 0u, D);
    ST(t2 = mt_();)
    EPI_LANE();
    const int grow = b * 8 + 2 * wm + (lane >> 5);
    u32x4 gx[4], gw[4];
#pragma unroll
    for (int i = 0; i < 4; ++i) {
      gx[i] = *(const u32x4*)(x + grow * DM + i * 256 + (lane & 31) * 8);
      gw[i] = *(const u32x4*)(gatew + i * 256 + (lane & 31) * 8);
    }
#pragma unroll
    for (int mt = 0; mt < 4; ++mt) ST16(pack8(acc[mt][0], acc[mt][1]), WS_QF + (c * 128 + m16 + mt) * 1024);
    const int j0 = 7 * c + ((c & 1) ? 3 : 0);
    const unsigned hp = WS_AP + ((j0 >> 1) * 128 + m16) * 1024;
#pragma unroll
    for (int mt = 0; mt < 4; ++mt) {
      const f32x4 h0 = geglu4(acc[mt][2], acc[mt][6]), h1 = geglu4(acc[mt][3], acc[mt][7]);
      ST16(pack8(h0, h1), hp + mt * 1024);
      const f32x4 h2 = geglu4(acc[mt][4], acc[mt][8]), h3 = geglu4(acc[mt][5], acc[mt][9]);
      ST16(pack8(h2, h3), hp + 131072 + mt * 1024);
    }
    {
      float s = 0.f;
#pragma unroll
      for (int i = 0; i < 4; ++i)
#pragma unroll
        for (int e = 0; e < 4; ++e) {
          const unsigned pr = pk2(__uint_as_float(gx[i][e] << 16) * __uint_as_float(gw[i][e] << 16), __uint_as_float(gx[i][e] & 0xffff0000u) * __uint_as_float(gw[i][e] & 0xffff0000u));
          s += __uint_as_float(pr << 16) + __uint_as_float(pr & 0xffff0000u);
        }
#pragma unroll
      for (int o = 1; o < 32; o <<= 1) s += __shfl_xor(s, o);
      s = bfr(s);
      const float gv = bfr(1.0f / (1.0f + __expf(-s)));
      if ((lane & 31) == 0) ((float*)(ws + WS_G))[grow] = gv;
      if (wm == 0 && lane == 0) { ((unsigned*)(ws + WS_FLAGS))[(8 + b) * 16] = 0u; if (b < 8) ((unsigned*)(ws + WS_FLAGS))[b * 16] = 0u; }
    }
  }
  LGKM_BAR();
  {
    int tid = w * 64 + __builtin_amdgcn_mbcnt_hi(~0u, __builtin_amdgcn_mbcnt_lo(~0u, 0u)); asm volatile("" : "+v"(tid));
    const unsigned rd = lb + 65536 + tid * 16;
    const f32x4 s = *(LAS f32x4*)(rd) + *(LAS f32x4*)(rd + 8192) + *(LAS f32x4*)(rd + 16384) + *(LAS f32x4*)(rd + 24576);
    __builtin_amdgcn_raw_buffer_store_b64((u32x2){pk2(s[0], s[1]), pk2(s[2], s[3])}, rw, WS_KVP + ((c * 8 + w) * 4 + (rb >> 1)) * 1024 + (tid & 63) * 16 + (rb & 1) * 8, 0, ST_AUX);
    if (tid < 16) {
      const unsigned rz = lb + 65536 + 32768 + tid * 16;
      const f32x4 zs = *(LAS f32x4*)(rz) + *(LAS f32x4*)(rz + 256) + *(LAS f32x4*)(rz + 512) + *(LAS f32x4*)(rz + 768);
      *(f32x4*)(ws + WS_ZP + ((size_t)(c * 8 + rb) * 16 + tid) * 16) = zs;
    }
  }
}

constexpr unsigned K2_SLOT = 16384, K2_KV = 131072, K2_LDS = 131072 + 4608;
constexpr int K2_NPIECE = 10;
constexpr int NS = 18, NH = 14;
#define VM_BAR(N) do { asm volatile("" ::: "memory"); __builtin_amdgcn_s_waitcnt(((N) & 15) | 0xF70 | (((N) >> 4) << 14)); __builtin_amdgcn_s_barrier(); asm volatile("" ::: "memory"); } while (0)
#define BAR() do { asm volatile("" ::: "memory"); __builtin_amdgcn_s_barrier(); asm volatile("" ::: "memory"); } while (0)

DI void wait_ge(const unsigned* flag, unsigned need) {
  for (unsigned spin = 0; spin < (1u << 22); ++spin) {
    if (__hip_atomic_load((const GAS unsigned*)flag, __ATOMIC_RELAXED, __HIP_MEMORY_SCOPE_AGENT) >= need) break;
    __builtin_amdgcn_s_sleep(2);
  }
}
#define lda(d, srd, voff, soff) d = __builtin_bit_cast(bf16x8, __builtin_amdgcn_raw_buffer_load_b128(srd, voff, soff, 16))

__global__ __launch_bounds__(768, 3) void k2_kernel(const uint16_t* __restrict__ wdown, const uint16_t* __restrict__ wo, char* __restrict__ ws,
                                                    uint16_t* __restrict__ out) {
  extern __shared__ __attribute__((aligned(16))) char smem[];
  const unsigned lb = (unsigned)(size_t)(LAS char*)smem;
  const int lane = threadIdx.x & 63;
  const int w = __builtin_amdgcn_readfirstlane(threadIdx.x >> 6);
  const int g = lane >> 4;
  const int b = blockIdx.x, xj = b & 7, bi = b >> 3;
  const int kq = xj >> 1;
  const int mt = 4 * (xj & 1) + (bi >> 3), nt128 = bi & 7;
  const int tile = mt * 8 + nt128;
  const int oc = 4 * xj + (bi & 3), orb = bi >> 2;
  unsigned* const flags = (unsigned*)(ws + WS_FLAGS);
  const float* const gs = (const float*)(ws + WS_G);
  auto tix = [&](int s) -> int { return s < NH ? NH * kq + s : 56 - NH + 4 * kq + s; };

  if (w >= 8) {
    const int sw = w - 8;
    const u32x4 sd = mk_srd(wdown, DF * DM * 2), so_ = mk_srd(wo, DM * DM * 2);
    const unsigned vb0 = (16 * sw + g) * 2048 + nt128 * 256 + (((lane & 15) ^ (g << 1)) << 4), vb1 = (vb0 ^ 128) + 4 * 2048;
    auto stage = [&](int s) {
      if (s < NS) {
        u32x4 r;
#pragma unroll
        for (int j = 0; j < 4; ++j) r[j] = s >= NH ? so_[j] : sd[j];
        const unsigned so = (s >= NH ? 4 * kq + s - NH : NH * kq + s) * 131072, dst = lb + (s & 7) * K2_SLOT + sw * 4096;
#pragma unroll
        for (int i = 0; i < 4; ++i) if (ABL != 12) dma16(r, dst + i * 1024, (i & 1) ? vb1 : vb0, so + (i >> 1) * 16384);
      }
    };
    stage(0); stage(1); stage(2); stage(3);
    bf16x8 qf[4]; float go[4];
    const bf16x8 wp0 = *(const bf16x8*)(ws + WS_WPF + oc * 2048 + lane * 16), wp1 = *(const bf16x8*)(ws + WS_WPF + oc * 2048 + 1024 + lane * 16);
#pragma unroll
    for (int i = 0; i < 4; ++i) {
      qf[i] = *(const bf16x8*)(ws + WS_QF + ((size_t)oc * 128 + orb * 16 + 4 * sw + i) * 1024 + lane * 16);
      go[i] = gs[(orb * 16 + 4 * sw + i) * 16 + (lane & 15)];
    }
    bf16x8 pf0, pf1;
    auto fm = [&](int i) { f32x4 pp0, pp1, pn0, pn1; featmap(qf[i], wp0, wp1, pp0, pp1, pn0, pn1); pf0 = pack8(pp0, pp1); pf1 = pack8(pn0, pn1); };
    VM_BAR(K2_NPIECE + 8);
    stage(4); stage(5);
    bf16x8 ka[2][2], za[2];
#pragma unroll
    for (int ks = 0; ks < 2; ++ks) {
      ka[0][ks] = ldsrd(lb + K2_KV + (0 + ks) * 1024 + lane * 16);
      ka[1][ks] = ldsrd(lb + K2_KV + (2 + ks) * 1024 + lane * 16);
      za[ks] = ldsrd(lb + K2_KV + 4096 + ks * 64 + g * 16);
    }
    const auto rs = __builtin_amdgcn_make_buffer_rsrc((void*)(ws + WS_AP), 0, 144u * 131072u, 0x00020000);
    auto fin = [&](int i) {
      const f32x4 z4 = {0.f, 0.f, 0.f, 0.f};
      f32x4 n0 = mfma16(ka[0][1], pf1, mfma16(ka[0][0], pf0, z4));
      f32x4 n1 = mfma16(ka[1][1], pf1, mfma16(ka[1][0], pf0, z4));
      f32x4 dn = mfma16(za[1], pf1, mfma16(za[0], pf0, z4));
      const float sc = go[i] * __builtin_amdgcn_rcpf(dn[0] + 1e-6f);
      n0 *= sc; n1 *= sc;
      __builtin_amdgcn_raw_buffer_store_b128(__builtin_bit_cast(u32x4, pack8(n0, n1)), rs, lane * 16 + ((112 + oc) * 128 + orb * 16 + 4 * sw + i) * 1024, 0, 16  );
      asm volatile("" ::: "memory");
    };
    fm(0); fin(0); fm(1); fin(1); SB();
    VM_BAR(8);
    stage(6); stage(7);
    fm(2); fin(2); fm(3); fin(3); SB();
    VM_BAR(8);
    stage(8); stage(9);
    VM_BAR(8);
    if (threadIdx.x == 512) __hip_atomic_fetch_add((GAS unsigned*)(flags + orb * 16), 1u, __ATOMIC_RELAXED, __HIP_MEMORY_SCOPE_AGENT);
    for (int k = 3; k < NS / 2 - 1; ++k) {
      stage(2 * k + 4); stage(2 * k + 5);
      if (k == 4 && sw == 3) {
        if (lane == 0) wait_ge(flags + mt * 16, 32u);
        asm volatile("s_waitcnt vmcnt(0)" ::: "memory");
      }
      if (2 * k + 4 < NS) VM_BAR(8); else VM_BAR(0);
    }
    BAR();
    if (sw == 0) {
      if (lane == 0) __hip_atomic_store((GAS unsigned*)(flags + (8 + tile * 4 + kq) * 16), 1u, __ATOMIC_RELAXED, __HIP_MEMORY_SCOPE_AGENT);
      if (lane < 4 && lane != kq) wait_ge(flags + (8 + tile * 4 + lane) * 16, 1u);
      asm volatile("s_waitcnt vmcnt(0)" ::: "memory");
      BAR();
    }
    return;
  }

  auto compute = [&](auto grp_) {
  constexpr int GRP = decltype(grp_)::value;
  ST(unsigned long long wsum = 0, r0 = rt_(), t0 = mt_();)
  const int q = (lane & 15) >> 2, p = lane & 3;
  const auto sa = __builtin_amdgcn_make_buffer_rsrc((void*)(ws + WS_AP), 0, 144u * 131072u, 0x00020000);
  const unsigned va = lane * 16;
  const unsigned abase = (mt * 16 + 2 * w) * 1024;
  bf16x8 ar[4][2];
  { const unsigned a0 = abase + tix(0) * 262144, a1 = abase + tix(1) * 262144;
    lda(ar[0][0], sa, va, a0); lda(ar[0][1], sa, va, a0 + 1024);
    lda(ar[1][0], sa, va, a0 + 131072); lda(ar[1][1], sa, va, a0 + 131072 + 1024);
    lda(ar[2][0], sa, va, a1); lda(ar[2][1], sa, va, a1 + 1024); }
  const float gsc0 = gs[mt * 256 + w * 32 + (lane & 15)], gsc1 = gs[mt * 256 + w * 32 + 16 + (lane & 15)];
  {
    const char* kp = ws + WS_KVP + (size_t)(oc * 8 + w) * 4096 + lane * 16;
    u32x4 kvl[4];
#pragma unroll
    for (int r = 0; r < 4; ++r) kvl[r] = *(const u32x4*)(kp + r * 1024);
    const char* zp = ws + WS_ZP + ((size_t)oc * 8 * 16 + 2 * g * 16 + (lane & 15)) * 16;
    f32x4 zs = *(const f32x4*)zp + *(const f32x4*)(zp + 256);
    f32x4 kvs = {0.f, 0.f, 0.f, 0.f};
#pragma unroll
    for (int r = 0; r < 4; ++r) kvs += unpk_lo(kvl[r]) + unpk_hi(kvl[r]);
    *(LAS u32x2*)(lb + K2_KV + ((w & 1) * 2 + (w >> 2)) * 1024 + lane * 16 + ((w >> 1) & 1) * 8) = (u32x2){pk2(kvs[0], kvs[1]), pk2(kvs[2], kvs[3])};
    float z0 = zs[0], z1 = zs[1], z2 = zs[2], z3 = zs[3]; XRED2(z0, z1, fadd_); XRED2(z2, z3, fadd_);
    if (w == 0) *(LAS u32x2*)(lb + K2_KV + 4096 + ((lane >> 3) & 1) * 64 + (lane & 3) * 16 + ((lane >> 2) & 1) * 8) = (u32x2){pk2(z0, z1), pk2(z2, z3)};
  }
  f32x4 acc[2][8];
#pragma unroll
  for (int m = 0; m < 2; ++m)
#pragma unroll
    for (int n = 0; n < 8; ++n) acc[m][n] = (f32x4){0.f, 0.f, 0.f, 0.f};
  const unsigned bb0 = lb + (4 * g + q) * 256 + (((4 * (g & 1) + q) << 1 | (p >> 1)) << 4) + (p & 1) * 8;
  unsigned ba[8];
#pragma unroll
  for (int n = 0; n < 8; ++n) ba[n] = bb0 ^ (n << 5);
  unsigned sp2 = 0;
  bf16x8 bw[8];
  auto ldB = [&](int par, int u) -> bf16x8 { const unsigned a = ba[u & 7] + par * K2_SLOT + (u >> 3) * 8192; return trrd2(a, a + 4096); };
  __builtin_amdgcn_s_waitcnt(0xC07F);
  BAR();
  SB(); MFMA_FENCE();
  ST(unsigned long long t1 = mt_();)
#pragma unroll
  for (int u = 0; u < 6; ++u) bw[u] = ldB(0, u);
  auto step = [&](int s, auto par_, auto ia0_, auto ia1_, auto nxt_) {
    constexpr int par = decltype(par_)::value;
    constexpr bool ia0 = decltype(ia0_)::value, ia1 = decltype(ia1_)::value, nxt = decltype(nxt_)::value;
    const unsigned an1 = abase + tix(s + 1) * 262144 + 131072, an2 = abase + tix(s + 2) * 262144;
#pragma unroll
    for (int u = 0; u < 16; ++u) {
      if (u == 10 && nxt && par == 1) {
        BAR();
        const unsigned nx = (sp2 + 32768) & 98304, dx = sp2 ^ nx; sp2 = nx;
#pragma unroll
        for (int n = 0; n < 8; ++n) { ba[n] ^= dx; asm volatile("" : "+v"(ba[n])); }
      }
      if (u < 10) bw[(u + 6) & 7] = ldB(par, u + 6);
      else if (nxt) bw[(u + 6) & 7] = ldB(par ^ 1, u - 10);
      if (ia0 && u < 2) lda(ar[(2 * par + 3) & 3][u], sa, va, an1 + u * 1024);
      if (ia1 && (u == 8 || u == 9)) lda(ar[2 * par][u - 8], sa, va, an2 + (u - 8) * 1024);
      if ((u & 1) == 0 && (u < 10 || nxt)) __builtin_amdgcn_s_waitcnt(0xCA7F);
#pragma unroll
      for (int m = 0; m < 2; ++m) mfma_ip(acc[m][u & 7], bw[u & 7], ar[2 * par + (u >> 3)][m]);
      SB();
    }
  };
#pragma unroll 1
  for (int s = 0; s < NH; s += 2) { step(s, I0{}, I1{}, I1{}, I1{}); step(s + 1, I1{}, I1{}, I1{}, I1{}); }
  {
    MFMA_FENCE(); SB();
    const float s0 = bfr(1.0f - gsc0), s1 = bfr(1.0f - gsc1);
#pragma unroll
    for (int n = 0; n < 8; ++n) { acc[0][n] *= s0; acc[1][n] *= s1; }
    SB(); MFMA_FENCE();
  }
  step(NH, I0{}, I1{}, I1{}, I1{}); step(NH + 1, I1{}, I1{}, I1{}, I1{});
  ST(wsum = mt_();)
  step(NH + 2, I0{}, I1{}, I0{}, I1{});
  step(NH + 3, I1{}, I0{}, I0{}, I0{});
  MFMA_FENCE(); SB();
  ST(unsigned long long t2 = mt_();)
  const auto rp = __builtin_amdgcn_make_buffer_rsrc((void*)(ws + WS_PART), 0, 64u * 262144u, 0x00020000);
  const unsigned ex = lb + 32768;
  if ((w >> 1) == kq) {
#pragma unroll
    for (int m = 0; m < 2; ++m)
#pragma unroll
      for (int n = 0; n < 8; ++n) *(LAS f32x4*)(ex + (((w & 1) * 2 + m) * 8 + n) * 1024 + va) = acc[m][n];
  } else {
#pragma unroll
    for (int m = 0; m < 2; ++m)
#pragma unroll
      for (int n = 0; n < 4; ++n)
        __builtin_amdgcn_raw_buffer_store_b128(__builtin_bit_cast(u32x4, pack8(acc[m][2 * n], acc[m][2 * n + 1])), rp, va + (tile * 4 + kq) * 65536 + w * 8192 + m * 4096 + n * 1024, 0, 16);
  }
  ST(unsigned long long e0 = mt_();)
  WAIT_BAR(0);
  ST(unsigned long long e1 = mt_();)
  f32x4 fin[4];
#pragma unroll
  for (int n = 0; n < 4; ++n) fin[n] = *(LAS f32x4*)(ex + ((w >> 1) * 8 + 4 * (w & 1) + n) * 1024 + va);
  BAR();
  ST(unsigned long long e2 = mt_();)
  {
    u32x4 part[3][2];
#pragma unroll
    for (int j = 0; j < 3; ++j) {
      const int src = (kq + 1 + j) & 3;
      const unsigned po = (tile * 4 + src) * 65536 + (2 * kq + (w >> 2)) * 8192 + ((w >> 1) & 1) * 4096 + 2 * (w & 1) * 1024;
      part[j][0] = __builtin_amdgcn_raw_buffer_load_b128(rp, va, po, 16);
      part[j][1] = __builtin_amdgcn_raw_buffer_load_b128(rp, va, po + 1024, 16);
    }
    uint16_t* orow = out + (size_t)(mt * 256 + kq * 64 + (w >> 1) * 16 + (lane & 15)) * DM + nt128 * 128 + (w & 1) * 64 + 4 * g;
#pragma unroll
    for (int n = 0; n < 2; ++n) {
      const f32x4 v0 = fin[2 * n] + (unpk_lo(part[0][n]) + unpk_lo(part[1][n]) + unpk_lo(part[2][n]));
      const f32x4 v1 = fin[2 * n + 1] + (unpk_hi(part[0][n]) + unpk_hi(part[1][n]) + unpk_hi(part[2][n]));
      *(u32x2*)(orow + n * 32) = (u32x2){pk2(v0[0], v0[1]), pk2(v0[2], v0[3])};
      *(u32x2*)(orow + n * 32 + 16) = (u32x2){pk2(v1[0], v1[1]), pk2(v1[2], v1[3])};
    }
  }
  };
  compute(I0{});
}

extern "C" void kernel_launch(void* const* d_in, const int* in_sizes, int n_in, void* d_out, int out_size, void* d_ws, size_t ws_size,
                              hipStream_t stream) {
  const uint16_t* x = (const uint16_t*)d_in[0];
  const uint16_t* wqkv = (const uint16_t*)d_in[1];
  const uint16_t* wphi = (const uint16_t*)d_in[2];
  const uint16_t* wo = (const uint16_t*)d_in[3];
  const uint16_t* wg = (const uint16_t*)d_in[5];
  const uint16_t* wu = (const uint16_t*)d_in[6];
  const uint16_t* wd = (const uint16_t*)d_in[7];
  const uint16_t* gw = (const uint16_t*)d_in[8];
  (void)hipFuncSetAttribute((const void*)k1_kernel, hipFuncAttributeMaxDynamicSharedMemorySize, K1_LDS);
  (void)hipFuncSetAttribute((const void*)k2_kernel, hipFuncAttributeMaxDynamicSharedMemorySize, K2_LDS);
  k1_kernel<<<256, 512, K1_LDS, stream>>>(x, wqkv, wphi, wg, wu, gw, wd, wo, (char*)d_ws);
  k2_kernel<<<256, 768, K2_LDS, stream>>>(wd, wo, (char*)d_ws, (uint16_t*)d_out);
}
```

```cpp
#define NO_CODE_PF
#include <hip/hip_runtime.h>
#include <hip/hip_bf16.h>
#include <stdint.h>
#include <type_traits>
#pragma clang diagnostic ignored "-Wint-to-pointer-cast"

typedef short bf16x8 __attribute__((ext_vector_type(8)));
typedef short s16x4 __attribute__((ext_vector_type(4)));
typedef float f32x4 __attribute__((ext_vector_type(4)));
typedef float f32x2 __attribute__((ext_vector_type(2)));
typedef unsigned u32x4 __attribute__((ext_vector_type(4)));
typedef unsigned u32x2 __attribute__((ext_vector_type(2)));
typedef __bf16 bf16x2_t __attribute__((ext_vector_type(2)));
#define LAS __attribute__((address_space(3)))
#define GAS __attribute__((address_space(1)))
#define DI __device__ __forceinline__

constexpr int SEQ = 2048, DM = 1024, DF = 3584;
constexpr size_t WS_FLAGS = 0;
constexpr size_t WS_G     = 32768;
constexpr size_t WS_ZP    = 65536;
constexpr size_t WS_KVP   = 262144;
constexpr size_t WS_WPF   = 3u << 20;
constexpr size_t WS_QF    = 4u << 20;
constexpr size_t WS_AP    = 16u << 20;
constexpr size_t WS_PART  = 40u << 20;

#define ABL 0
DI f32x4 mfma16(bf16x8 a, bf16x8 b, f32x4 c) { return __builtin_amdgcn_mfma_f32_16x16x32_bf16(a, b, c, 0, 0, 0); }
DI void mfma_ip(f32x4& c, bf16x8 a, bf16x8 b) { asm volatile("v_mfma_f32_16x16x32_bf16 %0, %1, %2, %0" : "+v"(c) : "v"(a), "v"(b)); }
#define MFMA_FENCE() asm volatile("s_nop 7\n\ts_nop 7" ::: "memory")
DI s16x4 trrd(unsigned a) { return __builtin_amdgcn_ds_read_tr16_b64_v4i16((LAS s16x4*)a); }
DI bf16x8 trrd2(unsigned a0, unsigned a1) { s16x4 lo = trrd(a0), hi = trrd(a1); return __builtin_shufflevector(lo, hi, 0, 1, 2, 3, 4, 5, 6, 7); }
DI bf16x8 ldsrd(unsigned a) { return *(LAS bf16x8*)a; }
DI unsigned pk2(float lo, float hi) { f32x2 v = {lo, hi}; bf16x2_t b = __builtin_convertvector(v, bf16x2_t); return __builtin_bit_cast(unsigned, b); }
DI bf16x8 pack8(f32x4 a, f32x4 b) { u32x4 r = {pk2(a[0], a[1]), pk2(a[2], a[3]), pk2(b[0], b[1]), pk2(b[2], b[3])}; return __builtin_bit_cast(bf16x8, r); }
DI f32x4 unpk_lo(u32x4 v) { return (f32x4){__uint_as_float(v[0] << 16), __uint_as_float(v[0] & 0xffff0000u), __uint_as_float(v[1] << 16), __uint_as_float(v[1] & 0xffff0000u)}; }
DI f32x4 unpk_hi(u32x4 v) { return (f32x4){__uint_as_float(v[2] << 16), __uint_as_float(v[2] & 0xffff0000u), __uint_as_float(v[3] << 16), __uint_as_float(v[3] & 0xffff0000u)}; }
DI float bfr(float x) { unsigned u = pk2(x, x); return __uint_as_float(u << 16); }
DI u32x4 mk_srd(const void* p, unsigned bytes) {
  uint64_t a = (uint64_t)p;
  u32x4 r;
  r[0] = __builtin_amdgcn_readfirstlane((unsigned)a);
  r[1] = __builtin_amdgcn_readfirstlane((unsigned)(a >> 32) & 0xffffu);
  r[2] = __builtin_amdgcn_readfirstlane(bytes);
  r[3] = 0x00020000u;
  return r;
}
DI void pfetch(u32x4 srd, unsigned lds, unsigned voff, unsigned soff) {
  asm volatile("s_mov_b32 m0, %0\n\ts_nop 0\n\tbuffer_load_dword %1, %2, %3 offen lds" ::"s"(lds), "v"(voff), "s"(srd), "s"(soff) : "memory");
}
#define CODE_SRD(srd, len, SYM, END) do { unsigned long long pc_; int o_; \
    asm volatile("s_getpc_b64 %0\n.Lpc%=:\n\ts_mov_b32 %1, " SYM "-.Lpc%=\n\ts_mov_b32 %2, " END "-" SYM : "=s"(pc_), "=s"(o_), "=s"(len)); \
    srd = mk_srd((const void*)(pc_ + (long long)o_), len); } while (0)
#define K1_SYM "_Z9k1_kernelPKtS0_S0_S0_S0_S0_S0_S0_Pc"
#define K2_SYM "_Z9k2_kernelPKtS0_PcPt"
DI void dma16(u32x4 srd, unsigned lds, unsigned voff, unsigned soff) {
  asm volatile("s_mov_b32 m0, %0\n\ts_nop 0\n\tbuffer_load_dwordx4 %1, %2, %3 offen lds" ::"s"(lds), "v"(voff), "s"(srd), "s"(soff) : "memory");
}
#define WAIT_BAR(N) do { asm volatile("" ::: "memory"); __builtin_amdgcn_s_waitcnt(((N) & 15) | 0x70 | (((N) >> 4) << 14)); __builtin_amdgcn_s_barrier(); asm volatile("" ::: "memory"); } while (0)
#define LGKM_BAR() do { asm volatile("" ::: "memory"); __builtin_amdgcn_s_waitcnt(0xC07F); __builtin_amdgcn_s_barrier(); asm volatile("" ::: "memory"); } while (0)
#define SB() __builtin_amdgcn_sched_barrier(0)
#define ST(...)
#define WAIT_BAR_T(N) WAIT_BAR(N)

DI float fmx(float a, float b) { return __builtin_amdgcn_fmed3f(a, b, __builtin_inff()); }
#define XRED2(a, b, OP) do { \
    auto r1_ = __builtin_amdgcn_permlane16_swap(__float_as_uint(a), __float_as_uint(b), false, false); \
    float m_ = OP(__uint_as_float(r1_[0]), __uint_as_float(r1_[1])); \
    auto r2_ = __builtin_amdgcn_permlane32_swap(__float_as_uint(m_), __float_as_uint(m_), false, false); \
    m_ = OP(__uint_as_float(r2_[0]), __uint_as_float(r2_[1])); \
    auto r3_ = __builtin_amdgcn_permlane16_swap(__float_as_uint(m_), __float_as_uint(m_), false, false); \
    a = __uint_as_float(r3_[0]); b = __uint_as_float(r3_[1]); } while (0)
DI float fadd_(float a, float b) { return a + b; }
DI f32x4 exp2v(f32x4 v) { return (f32x4){__builtin_amdgcn_exp2f(v[0]), __builtin_amdgcn_exp2f(v[1]), __builtin_amdgcn_exp2f(v[2]), __builtin_amdgcn_exp2f(v[3])}; }

DI void featmap(bf16x8 fr, bf16x8 wp0, bf16x8 wp1, f32x4& pp0, f32x4& pp1, f32x4& pn0, f32x4& pn1) {
  const f32x4 z4 = {0.f, 0.f, 0.f, 0.f};
  constexpr float L2E = 1.4426950408889634f;
  const f32x4 t0 = mfma16(wp0, fr, z4) * L2E, t1 = mfma16(wp1, fr, z4) * L2E;
  float mx = fmaxf(fmaxf(fmaxf(t0[0], t0[1]), fmaxf(t0[2], t0[3])), fmaxf(fmaxf(t1[0], t1[1]), fmaxf(t1[2], t1[3])));
  float nm = -fminf(fminf(fminf(t0[0], t0[1]), fminf(t0[2], t0[3])), fminf(fminf(t1[0], t1[1]), fminf(t1[2], t1[3])));
  XRED2(mx, nm, fmx);
  pp0 = exp2v(t0 - mx); pp1 = exp2v(t1 - mx);
  pn0 = exp2v(-nm - t0); pn1 = exp2v(-nm - t1);
  const f32x4 s4p = pp0 + pp1, s4n = pn0 + pn1;
  float sp = (s4p[0] + s4p[1]) + (s4p[2] + s4p[3]), sn = (s4n[0] + s4n[1]) + (s4n[2] + s4n[3]);
  XRED2(sp, sn, fadd_);
  const float ip = __builtin_amdgcn_rcpf(sp), in = __builtin_amdgcn_rcpf(sn);
  pp0 *= ip; pp1 *= ip; pn0 *= in; pn1 *= in;
}

DI f32x4 geglu4(f32x4 g, f32x4 u) {
  constexpr float C1 = -2.0f * 0.7978845608028654f * 1.4426950408889634f, C2 = C1 * 0.044715f;
  const f32x4 d = exp2v(g * (g * g * C2 + C1)) + 1.0f;
  const f32x4 r = {__builtin_amdgcn_rcpf(d[0]), __builtin_amdgcn_rcpf(d[1]), __builtin_amdgcn_rcpf(d[2]), __builtin_amdgcn_rcpf(d[3])};
  return (g * u) * r;
}
#define ST_AUX 16
DI void prio_if(int hi) { asm volatile("s_setprio 0\n\ts_cmp_eq_u32 %0, 0\n\ts_cbranch_scc1 1f\n\ts_setprio 1\n1:" ::"s"(hi) : "scc"); }
#define PRIO2 0
#define PRIO1 0
using I0 = std::integral_constant<int, 0>; using I1 = std::integral_constant<int, 1>;

#define K1STG(x) x
constexpr unsigned K1_X0 = 0, K1_XS = K1STG(32768), K1_W0 = 65536, K1_WS = K1STG(40960), K1_WG = 12288, K1_WU = 26624, K1_PHI = 147456, K1_PF = 149504, K1_LDS = 151552 + 1024;

#define PFU 4
struct K1Dma {
  u32x4 sa, sb, sc;
  unsigned co[4], cc[2];
  unsigned lo[10];
  unsigned stab, stc;
};

template <int WN>
DI void k1_body(unsigned long long& wsum, f32x4 (&acc)[4][10], const unsigned lb, const int wm, const int lane,
                const u32x4 sx, const unsigned v0, const unsigned v1, const unsigned v2, const unsigned vxa, const unsigned vxb, const K1Dma& D) {
  const int g = lane >> 4, q = (lane & 15) >> 2, p = lane & 3, fr = lane & 15;
  const unsigned xrow = wm * 64 + fr;
  const unsigned xb0 = lb + K1_X0 + xrow * 128 + ((g ^ ((xrow >> 1) & 7)) << 4);
  const unsigned xb1 = xb0 ^ 64;
  const unsigned qb0 = lb + K1_W0 + (8 * g + q) * 192 + ((0 ^ (g & 1)) << 5) + p * 8;
  const unsigned qb1 = lb + K1_W0 + (8 * g + q) * 192 + ((1 ^ (g & 1)) << 5) + p * 8;
  const unsigned gb0 = lb + K1_W0 + K1_WG + (8 * g + 4 * (0 ^ (g & 1)) + q) * 224 + p * 8;
  const unsigned gb1 = lb + K1_W0 + K1_WG + (8 * g + 4 * (1 ^ (g & 1)) + q) * 224 + p * 8;
  const unsigned xl = lb + K1_X0 + wm * 8192, wl = lb + K1_W0;
  unsigned sxo = 128, tab = D.stab, tc = D.stc;
  int pt = 3;

  bf16x8 xf[2][4], wf[4];
  auto ldW = [&](int st, int u) -> bf16x8 {
    const int ks = u / 10, i = u % 10;
    const unsigned so = st * K1_WS;
    constexpr int NQ = WN == 0 ? 4 : 2;
    constexpr int NG = WN == 0 ? 3 : 4;
    constexpr int P0 = WN == 0 ? 4 : 0;
    if (i < NQ) {
      const int ptl = WN == 0 ? 1 + (i >> 1) : 0, nh = i & 1;
      const unsigned b = (nh ? qb1 : qb0) + so + ks * 32 * 192 + ptl * 64;
      return trrd2(b, b + 4 * 192);
    } else {
      const int j = i - NQ, up = j / NG, P = P0 + j % NG;
      const unsigned o = so + ks * 32 * 224 + up * (K1_WU - K1_WG) + P * 32;
      return trrd2(gb0 + o, gb1 + o);
    }
  };
  auto ldX = [&](int st, int ks, int mt) -> bf16x8 { return ldsrd((ks ? xb1 : xb0) + st * K1_XS + mt * 2048); };
  auto mma = [&](int u) {
    const int ks = u / 10, i = u % 10;
#pragma unroll
    for (int mt = 0; mt < 4; ++mt) {
      if (WN == 0 && (i == 2 || i == 3)) mfma_ip(acc[mt][i], xf[ks][mt], wf[u & 3]);
      else mfma_ip(acc[mt][i], wf[u & 3], xf[ks][mt]);
    }
  };
  auto dmaX = [&](int st, int j) { if (ABL != 10) dma16(sx, xl + st * K1_XS + j * 1024, (j & 1) ? vxb : vxa, sxo + j * 16384); };
  auto dmaW = [&](int st, int s) {
    if (ABL == 10) return;
    if (s < 8) dma16((s & 2) ? D.sb : D.sa, wl + st * K1_WS + D.lo[s], s < 4 ? v0 : v1, tab + D.co[s & 3]);
    else dma16(D.sc, wl + st * K1_WS + D.lo[s], v2, tc + D.cc[s - 8]);
  };
  auto warm = [&](int k) {   if (ABL == 10) return;
    const unsigned i0 = (unsigned)(pt - (int)D.co[2 * k]), i = k ? (i0 * D.cc[0]) >> 7 : i0;
    const bool on = i <= D.co[2 * k + 1] && (!k || i * D.cc[1] == i0);
    u32x4 r = k ? D.sb : D.sa;
    r[2] = on ? r[2] : 0u;
    pfetch(r, lb + K1_PF + wm * 512 + k * 256, k ? v1 : v0, (on ? i : 0u) * (k ? D.stc : D.stab));
  };

#pragma unroll
  for (int mt = 0; mt < 4; ++mt) xf[0][mt] = ldX(0, 0, mt);
  wf[0] = ldW(0, 0); wf[1] = ldW(0, 1); wf[2] = ldW(0, 2);
  SB(); MFMA_FENCE();

  auto step = [&](auto st_, auto dox_, auto dow_) {
    constexpr int st = decltype(st_)::value;
    constexpr bool dox = decltype(dox_)::value, dow = decltype(dow_)::value;
#pragma unroll
    for (int u = 0; u < 17; ++u) {
      if (PRIO1 && ((u + 2) % (PRIO1 ? PRIO1 : 1)) == 0) { if ((((u + 2) / (PRIO1 ? PRIO1 : 1)) & 1) ^ WN) __builtin_amdgcn_s_setprio(1); else __builtin_amdgcn_s_setprio(0); }
      wf[(u + 3) & 3] = ldW(st, u + 3);
      if (u >= 2 && u < 6) xf[1][u - 2] = ldX(st, 1, u - 2);
      if (WN == 0 && dox) {
        if (u < 3) { dmaW(st ^ 1, 4 + 2 * u); dmaW(st ^ 1, 5 + 2 * u); }
        else if (u < 7) { dmaX(st ^ 1, 2 * (u - 3)); dmaX(st ^ 1, 2 * (u - 3) + 1); }
      }
      if (WN == 1 && dow && (u == PFU || u == PFU + 2)) warm(u != PFU);
      mma(u);
      SB();
    }
    if (WN == 0) { sxo += 128; tab += D.stab; tc += D.stc; } else ++pt;
    mma(17);
    SB();
    if (WN == 0) WAIT_BAR_T(0); else WAIT_BAR_T(63);
    if (dox) {
#pragma unroll
      for (int mt = 0; mt < 4; ++mt) xf[0][mt] = ldX(st ^ 1, 0, mt);
      wf[0] = ldW(st ^ 1, 0); wf[1] = ldW(st ^ 1, 1);
    }
    if (PRIO1) __builtin_amdgcn_s_setprio(WN);
    if (WN == 0 && dow) { dmaW(st, 0); dmaW(st, 1); }
    mma(18);
    SB();
    if (dox) wf[2] = ldW(st ^ 1, 2);
    if (WN == 0 && dow) { dmaW(st, 2); dmaW(st, 3); }
    mma(19);
    SB();
  };
  for (int t2 = 0; t2 < 7; ++t2) { step(I0{}, I1{}, I1{}); step(I1{}, I1{}, I1{}); }
  step(I0{}, I1{}, I0{});
  step(I1{}, I0{}, I0{});
  MFMA_FENCE(); SB();
}

__global__ __launch_bounds__(512, 2) void k1_kernel(const uint16_t* __restrict__ x, const uint16_t* __restrict__ wqkv, const uint16_t* __restrict__ wphi,
                                                    const uint16_t* __restrict__ wg, const uint16_t* __restrict__ wu, const uint16_t* __restrict__ gatew,
                                                    const uint16_t* __restrict__ wdown, const uint16_t* __restrict__ wo, char* __restrict__ ws) {
  extern __shared__ __attribute__((aligned(16))) char smem[];
  const unsigned lb = (unsigned)(size_t)(LAS char*)smem;
  unsigned long long wsum = 0; ST(unsigned long long r0 = rt_(); unsigned long long t0 = mt_(); unsigned long long t2 = 0;)
  const int tid = threadIdx.x, lane = tid & 63;
  const int w = __builtin_amdgcn_readfirstlane(tid >> 6);
  const int wn = w >> 2, wm = w & 3;
  const int b = blockIdx.x, xj = b & 7, bi = b >> 3;
  const int c = 4 * xj + (bi & 3), rb = bi >> 2;
  const u32x4 sx = mk_srd(x, SEQ * DM * 2);
  const u32x4 sq = mk_srd(wqkv, DM * 3072 * 2), sg = mk_srd(wg, DM * DF * 2), su = mk_srd(wu, DM * DF * 2);
  f32x4 acc[4][10];
#pragma unroll
  for (int m = 0; m < 4; ++m)
#pragma unroll
    for (int i = 0; i < 10; ++i) acc[m][i] = (f32x4){0.f, 0.f, 0.f, 0.f};
  K1Dma D;
  const int m16 = rb * 16 + wm * 4;
  const auto rw = __builtin_amdgcn_make_buffer_rsrc((void*)ws, 0, 64u << 20, 0x00020000);
#define EPI_LANE() int lane = __builtin_amdgcn_mbcnt_hi(~0u, __builtin_amdgcn_mbcnt_lo(~0u, 0u)); asm volatile("" : "+v"(lane)); const int g = lane >> 4;
#define ST16(val, so) __builtin_amdgcn_raw_buffer_store_b128(__builtin_bit_cast(u32x4, val), rw, lane * 16 + (unsigned)(so), 0, ST_AUX)
  ST(unsigned long long t1 = 0;)
  if (wn == 0) {
    const unsigned vxa = (rb * 256 + 64 * wm + (lane >> 3)) * 2048 + (((lane & 7) ^ (lane >> 4)) << 4), vxb = vxa ^ 64;
    const unsigned gtbl = (c & 1) ? 0x0216543u : 0x6543210u;
    auto pat = [&](bool isq, int pi) -> unsigned {
      const int ci = 64 * pi + lane, r = (ci * (isq ? 5462 : 4682)) >> 16, cc = ci - (isq ? 12 : 14) * r;
      const int o8 = (r >> 3) & 1;
      const unsigned vq = r * 6144 + ((cc >> 2) * 1024 + 32 * c + (((cc >> 1) & 1) ^ o8) * 16 + (cc & 1) * 8) * 2;
      const unsigned vg = (r ^ (o8 << 2)) * 7168 + (112 * c + 16 * ((gtbl >> (4 * (cc >> 1))) & 7) + 8 * (cc & 1)) * 2;
      return isq ? vq : vg;
    };
    const bool q01 = wm == 3, q2 = wm >= 2;
    const unsigned v0 = pat(q01, q01 ? 0 : 2 * wm), v1 = pat(q01, q01 ? 1 : 2 * wm + 1), v2 = pat(q2, q2 ? 2 : 6);
#pragma unroll
    for (int i = 0; i < 4; ++i) { D.sa[i] = q01 ? sq[i] : sg[i]; D.sb[i] = q01 ? sq[i] : su[i]; D.sc[i] = q2 ? sq[i] : (wm ? su[i] : sg[i]); }
    D.stab = q01 ? 64 * 6144 : 64 * 7168; D.stc = q2 ? 64 * 6144 : 64 * 7168;
    D.co[0] = 0; D.co[1] = q01 ? 16 * 6144 : 32 * 7168; D.co[2] = q01 ? 32 * 6144 : 0; D.co[3] = q01 ? 48 * 6144 : 32 * 7168;
#pragma unroll
    for (int h = 0; h < 2; ++h)
#pragma unroll
      for (int i = 0; i < 4; ++i) D.lo[4 * h + i] = q01 ? h * 1024 + i * 3072 : K1_WG + (2 * wm + h) * 1024 + i * 7168;
    D.cc[0] = q2 ? (wm - 2) * 32 * 6144 : 0; D.cc[1] = D.cc[0] + (q2 ? 16 * 6144 : 32 * 7168);
    D.lo[8] = q2 ? 2048 + (wm - 2) * 6144 : K1_WG + wm * 14336 + 6144; D.lo[9] = D.lo[8] + (q2 ? 3072 : 7168);
#pragma unroll
    for (int st = 0; st < 2; ++st) {
#pragma unroll
      for (int s = 0; s < (st ? (ABL == 10 ? 0 : 4) : 10); ++s) {
        if (s < 8) dma16((s & 2) ? D.sb : D.sa, lb + K1_W0 + st * K1_WS + D.lo[s], s < 4 ? v0 : v1, st * D.stab + D.co[s & 3]);
        else dma16(D.sc, lb + K1_W0 + st * K1_WS + D.lo[s], v2, st * D.stc + D.cc[s - 8]);
      }
    }
    WAIT_BAR(ABL == 10 ? 0 : 4);
    if (wm < 2) dma16(mk_srd(wphi, 32 * 32 * 32 * 2), lb + K1_PHI + wm * 1024, lane * 16, c * 2048 + wm * 1024);
    ST(t1 = mt_();)
    k1_body<0>(wsum, acc, lb, wm, lane, sx, v0, v1, v2, vxa, vxb, D);
    ST(t2 = mt_();)
    EPI_LANE();
    const int q = (lane & 15) >> 2, p = lane & 3;
    const unsigned wpb = lb + K1_PHI + (4 * g + q) * 64 + p * 8;
    const bf16x8 wp0 = trrd2(wpb, wpb + 16 * 64), wp1 = trrd2(wpb + 32, wpb + 16 * 64 + 32);
    if (rb == 0 && wm == 0) { ST16(wp0, WS_WPF + c * 2048); ST16(wp1, WS_WPF + c * 2048 + 1024); }
    const unsigned pkb = lb + wm * 8704;
    const unsigned pw = pkb + (lane & 15) * 136 + g * 8;
#pragma unroll
    for (int mt = 0; mt < 4; ++mt) {
      f32x4 pp0, pp1, pn0, pn1;
      featmap(pack8(acc[mt][0], acc[mt][1]), wp0, wp1, pp0, pp1, pn0, pn1);
      *(LAS u32x2*)(pw + mt * 16 * 136 + 0)  = (u32x2){pk2(pp0[0], pp0[1]), pk2(pp0[2], pp0[3])};
      *(LAS u32x2*)(pw + mt * 16 * 136 + 32) = (u32x2){pk2(pp1[0], pp1[1]), pk2(pp1[2], pp1[3])};
      *(LAS u32x2*)(pw + mt * 16 * 136 + 64) = (u32x2){pk2(pn0[0], pn0[1]), pk2(pn0[2], pn0[3])};
      *(LAS u32x2*)(pw + mt * 16 * 136 + 96) = (u32x2){pk2(pn1[0], pn1[1]), pk2(pn1[2], pn1[3])};
    }
    asm volatile("s_waitcnt lgkmcnt(0)" ::: "memory");
    f32x4 kv[4][2], zz[4];
#pragma unroll
    for (int ft = 0; ft < 4; ++ft) { kv[ft][0] = (f32x4){0.f, 0.f, 0.f, 0.f}; kv[ft][1] = kv[ft][0]; zz[ft] = kv[ft][0]; }
    const bf16x8 ones = {0x3f80, 0x3f80, 0x3f80, 0x3f80, 0x3f80, 0x3f80, 0x3f80, 0x3f80};
    const unsigned pr = pkb + (4 * g + q) * 136 + p * 8;
#pragma unroll
    for (int mp = 0; mp < 2; ++mp) {
      const bf16x8 va = pack8(acc[2 * mp][2], acc[2 * mp + 1][2]), vb = pack8(acc[2 * mp][3], acc[2 * mp + 1][3]);
#pragma unroll
      for (int ft = 0; ft < 4; ++ft) {
        const bf16x8 a = trrd2(pr + (32 * mp) * 136 + ft * 32, pr + (32 * mp + 16) * 136 + ft * 32);
        kv[ft][0] = mfma16(a, va, kv[ft][0]);
        kv[ft][1] = mfma16(a, vb, kv[ft][1]);
        zz[ft] = mfma16(a, ones, zz[ft]);
      }
    }
    const unsigned rd = lb + 65536 + wm * 8192 + lane * 16;
#pragma unroll
    for (int ft = 0; ft < 4; ++ft) {
      *(LAS f32x4*)(rd + (ft * 2 + 0) * 1024) = kv[ft][0];
      *(LAS f32x4*)(rd + (ft * 2 + 1) * 1024) = kv[ft][1];
    }
    { const int ft = lane & 3;
      const f32x4 zsel = ft == 0 ? zz[0] : (ft == 1 ? zz[1] : (ft == 2 ? zz[2] : zz[3]));
      *(LAS f32x4*)(lb + 65536 + 32768 + wm * 256 + ft * 64 + g * 16) = zsel; }
    const int jp = 7 * c + ((c & 1) ? 1 : 4), js = 7 * c + ((c & 1) ? 0 : 6);
    const unsigned hp = WS_AP + ((jp >> 1) * 128 + m16) * 1024, hs = WS_AP + ((js >> 1) * 128 + m16) * 1024 + (js & 1) * 8;
#pragma unroll
    for (int mt = 0; mt < 4; ++mt) {
      const f32x4 h0 = geglu4(acc[mt][4], acc[mt][7]), h1 = geglu4(acc[mt][5], acc[mt][8]), h2 = geglu4(acc[mt][6], acc[mt][9]);
      ST16(pack8(h0, h1), hp + mt * 1024);
      __builtin_amdgcn_raw_buffer_store_b64((u32x2){pk2(h2[0], h2[1]), pk2(h2[2], h2[3])}, rw, lane * 16 + hs + mt * 1024, 0, ST_AUX);
    }
  } else {
    {
      const unsigned vxa = (rb * 256 + 64 * wm + (lane >> 3)) * 2048 + (((lane & 7) ^ (lane >> 4)) << 4), vxb = vxa ^ 64;
#pragma unroll
      for (int j = 0; j < 8; ++j) dma16(sx, lb + K1_X0 + wm * 8192 + j * 1024, (j & 1) ? vxb : vxa, j * 16384);
    }
    const u32x4 sdn = mk_srd(wdown, DF * DM * 2), swo = mk_srd(wo, DM * DM * 2);
    const int r8 = 8 * rb + ((lane >> 2) & 7), l3 = lane & 3;
    const unsigned vx_ = (rb * 256 + (c & 3) * 64 + 32 * wm + (lane >> 1)) * 2048 + (lane & 1) * 64;
    const unsigned vg_ = r8 * 7168 + ((224 * c) & ~63) + l3 * 64;
    const unsigned vq_ = r8 * 6144 + (l3 == 3 ? 0 : l3) * 2048 + 64 * c;
    const unsigned v0 = wm < 2 ? vx_ : (wm == 2 ? vg_ : vq_);
    const unsigned v1 = wm == 2 ? vg_ : (wm < 2 ? (b * 7 + 4 * wm) * 4096 : b * 2 * 4096) + lane * 64;
#pragma unroll
    for (int i = 0; i < 4; ++i) { D.sa[i] = wm < 2 ? sx[i] : (wm == 2 ? sg[i] : sq[i]); D.sb[i] = wm < 2 ? sdn[i] : (wm == 2 ? su[i] : swo[i]); }
    D.stab = wm < 2 ? 128 : (wm == 2 ? 64 * 7168 : 64 * 6144); D.stc = wm == 2 ? 64 * 7168 : 4096;
    D.co[0] = 0; D.co[1] = 15; D.co[2] = wm == 2 ? 0 : (wm == 3 ? 6 : 4 + wm); D.co[3] = wm == 2 ? 15 : (wm == 3 ? 1 : 3 - wm);
    D.cc[0] = wm == 2 ? 128 : 43; D.cc[1] = wm == 2 ? 1 : 3;
    {
      u32x4 sb2 = D.sb; sb2[2] = wm == 2 ? sb2[2] : 0u;
#pragma unroll
      for (int t = 1; t < 3; ++t) { pfetch(D.sa, lb + K1_PF + wm * 512, v0, t * D.stab); pfetch(sb2, lb + K1_PF + wm * 512 + 256, v1, wm == 2 ? t * D.stc : 0u); }
    }
    WAIT_BAR(ABL == 4 || ABL == 5 ? 0 : 4);
    ST(t1 = mt_();)
    k1_body<1>(wsum, acc, lb, wm, lane, sx, v0, v1, 0u, 0u, 0u, D);
    ST(t2 = mt_();)
    EPI_LANE();
    const int grow = b * 8 + 2 * wm + (lane >> 5);
    u32x4 gx[4], gw[4];
#pragma unroll
    for (int i = 0; i < 4; ++i) {
      gx[i] = *(const u32x4*)(x + grow * DM + i * 256 + (lane & 31) * 8);
      gw[i] = *(const u32x4*)(gatew + i * 256 + (lane & 31) * 8);
    }
#pragma unroll
    for (int mt = 0; mt < 4; ++mt) ST16(pack8(acc[mt][0], acc[mt][1]), WS_QF + (c * 128 + m16 + mt) * 1024);
    const int j0 = 7 * c + ((c & 1) ? 3 : 0);
    const unsigned hp = WS_AP + ((j0 >> 1) * 128 + m16) * 1024;
#pragma unroll
    for (int mt = 0; mt < 4; ++mt) {
      const f32x4 h0 = geglu4(acc[mt][2], acc[mt][6]), h1 = geglu4(acc[mt][3], acc[mt][7]);
      ST16(pack8(h0, h1), hp + mt * 1024);
      const f32x4 h2 = geglu4(acc[mt][4], acc[mt][8]), h3 = geglu4(acc[mt][5], acc[mt][9]);
      ST16(pack8(h2, h3), hp + 131072 + mt * 1024);
    }
    {
      float s = 0.f;
#pragma unroll
      for (int i = 0; i < 4; ++i)
#pragma unroll
        for (int e = 0; e < 4; ++e) {
          const unsigned pr = pk2(__uint_as_float(gx[i][e] << 16) * __uint_as_float(gw[i][e] << 16), __uint_as_float(gx[i][e] & 0xffff0000u) * __uint_as_float(gw[i][e] & 0xffff0000u));
          s += __uint_as_float(pr << 16) + __uint_as_float(pr & 0xffff0000u);
        }
#pragma unroll
      for (int o = 1; o < 32; o <<= 1) s += __shfl_xor(s, o);
      s = bfr(s);
      const float gv = bfr(1.0f / (1.0f + __expf(-s)));
      if ((lane & 31) == 0) ((float*)(ws + WS_G))[grow] = gv;
      if (wm == 0 && lane == 0) { ((unsigned*)(ws + WS_FLAGS))[(8 + b) * 16] = 0u; if (b < 8) ((unsigned*)(ws + WS_FLAGS))[b * 16] = 0u; }
    }
  }
  LGKM_BAR();
  {
    int tid = w * 64 + __builtin_amdgcn_mbcnt_hi(~0u, __builtin_amdgcn_mbcnt_lo(~0u, 0u)); asm volatile("" : "+v"(tid));
    const unsigned rd = lb + 65536 + tid * 16;
    const f32x4 s = *(LAS f32x4*)(rd) + *(LAS f32x4*)(rd + 8192) + *(LAS f32x4*)(rd + 16384) + *(LAS f32x4*)(rd + 24576);
    __builtin_amdgcn_raw_buffer_store_b64((u32x2){pk2(s[0], s[1]), pk2(s[2], s[3])}, rw, WS_KVP + ((c * 8 + w) * 4 + (rb >> 1)) * 1024 + (tid & 63) * 16 + (rb & 1) * 8, 0, ST_AUX);
    if (tid < 16) {
      const unsigned rz = lb + 65536 + 32768 + tid * 16;
      const f32x4 zs = *(LAS f32x4*)(rz) + *(LAS f32x4*)(rz + 256) + *(LAS f32x4*)(rz + 512) + *(LAS f32x4*)(rz + 768);
      *(f32x4*)(ws + WS_ZP + ((size_t)(c * 8 + rb) * 16 + tid) * 16) = zs;
    }
    asm volatile("k1_code_mark:");
  }
}

constexpr unsigned K2_SLOT = 16384, K2_KV = 131072, K2_PF = 131072 + 4608, K2_LDS = 131072 + 4608 + 1024;
constexpr int K2_NPIECE = 10;
constexpr int NS = 18, NH = 14;
#define VM_BAR(N) do { asm volatile("" ::: "memory"); __builtin_amdgcn_s_waitcnt(((N) & 15) | 0xF70 | (((N) >> 4) << 14)); __builtin_amdgcn_s_barrier(); asm volatile("" ::: "memory"); } while (0)
#define BAR() do { asm volatile("" ::: "memory"); __builtin_amdgcn_s_barrier(); asm volatile("" ::: "memory"); } while (0)

DI void wait_ge(const unsigned* flag, unsigned need) {
  for (unsigned spin = 0; spin < (1u << 22); ++spin) {
    if (__hip_atomic_load((const GAS unsigned*)flag, __ATOMIC_RELAXED, __HIP_MEMORY_SCOPE_AGENT) >= need) break;
    __builtin_amdgcn_s_sleep(2);
  }
}
#define lda(d, srd, voff, soff) d = __builtin_bit_cast(bf16x8, __builtin_amdgcn_raw_buffer_load_b128(srd, voff, soff, 16))

__global__ __launch_bounds__(768, 3) void k2_kernel(const uint16_t* __restrict__ wdown, const uint16_t* __restrict__ wo, char* __restrict__ ws,
                                                    uint16_t* __restrict__ out) {
  extern __shared__ __attribute__((aligned(16))) char smem[];
  const unsigned lb = (unsigned)(size_t)(LAS char*)smem;
  const int lane = threadIdx.x & 63;
  const int w = __builtin_amdgcn_readfirstlane(threadIdx.x >> 6);
  const int g = lane >> 4;
  const int b = blockIdx.x, xj = b & 7, bi = b >> 3;
  const int kq = xj >> 1;
  const int mt = 4 * (xj & 1) + (bi >> 3), nt128 = bi & 7;
  const int tile = mt * 8 + nt128;
  const int oc = 4 * xj + (bi & 3), orb = bi >> 2;
  unsigned* const flags = (unsigned*)(ws + WS_FLAGS);
  const float* const gs = (const float*)(ws + WS_G);
  auto tix = [&](int s) -> int { return s < NH ? NH * kq + s : 56 - NH + 4 * kq + s; };

  if (w >= 8) {
    const int sw = w - 8;
    const u32x4 sd = mk_srd(wdown, DF * DM * 2), so_ = mk_srd(wo, DM * DM * 2);
    const unsigned vb0 = (16 * sw + g) * 2048 + nt128 * 256 + (((lane & 15) ^ (g << 1)) << 4), vb1 = (vb0 ^ 128) + 4 * 2048;
    auto stage = [&](int s) {
      if (s < NS) {
        u32x4 r;
#pragma unroll
        for (int j = 0; j < 4; ++j) r[j] = s >= NH ? so_[j] : sd[j];
        const unsigned so = (s >= NH ? 4 * kq + s - NH : NH * kq + s) * 131072, dst = lb + (s & 7) * K2_SLOT + sw * 4096;
#pragma unroll
        for (int i = 0; i < 4; ++i) if (ABL != 12) dma16(r, dst + i * 1024, (i & 1) ? vb1 : vb0, so + (i >> 1) * 16384);
      }
    };
    stage(0); stage(1); stage(2); stage(3);
    bf16x8 qf[4]; float go[4];
    const bf16x8 wp0 = *(const bf16x8*)(ws + WS_WPF + oc * 2048 + lane * 16), wp1 = *(const bf16x8*)(ws + WS_WPF + oc * 2048 + 1024 + lane * 16);
#pragma unroll
    for (int i = 0; i < 4; ++i) {
      qf[i] = *(const bf16x8*)(ws + WS_QF + ((size_t)oc * 128 + orb * 16 + 4 * sw + i) * 1024 + lane * 16);
      go[i] = gs[(orb * 16 + 4 * sw + i) * 16 + (lane & 15)];
    }
    bf16x8 pf0, pf1;
    auto fm = [&](int i) { f32x4 pp0, pp1, pn0, pn1; featmap(qf[i], wp0, wp1, pp0, pp1, pn0, pn1); pf0 = pack8(pp0, pp1); pf1 = pack8(pn0, pn1); };
    VM_BAR(K2_NPIECE + 8);
    stage(4); stage(5);
    bf16x8 ka[2][2], za[2];
#pragma unroll
    for (int ks = 0; ks < 2; ++ks) {
      ka[0][ks] = ldsrd(lb + K2_KV + (0 + ks) * 1024 + lane * 16);
      ka[1][ks] = ldsrd(lb + K2_KV + (2 + ks) * 1024 + lane * 16);
      za[ks] = ldsrd(lb + K2_KV + 4096 + ks * 64 + g * 16);
    }
    const auto rs = __builtin_amdgcn_make_buffer_rsrc((void*)(ws + WS_AP), 0, 144u * 131072u, 0x00020000);
    auto fin = [&](int i) {
      const f32x4 z4 = {0.f, 0.f, 0.f, 0.f};
      f32x4 n0 = mfma16(ka[0][1], pf1, mfma16(ka[0][0], pf0, z4));
      f32x4 n1 = mfma16(ka[1][1], pf1, mfma16(ka[1][0], pf0, z4));
      f32x4 dn = mfma16(za[1], pf1, mfma16(za[0], pf0, z4));
      const float sc = go[i] * __builtin_amdgcn_rcpf(dn[0] + 1e-6f);
      n0 *= sc; n1 *= sc;
      __builtin_amdgcn_raw_buffer_store_b128(__builtin_bit_cast(u32x4, pack8(n0, n1)), rs, lane * 16 + ((112 + oc) * 128 + orb * 16 + 4 * sw + i) * 1024, 0, 16  );
      asm volatile("" ::: "memory");
    };
    fm(0); fin(0); fm(1); fin(1); SB();
    VM_BAR(8);
    stage(6); stage(7);
    fm(2); fin(2); fm(3); fin(3); SB();
    VM_BAR(8);
    stage(8); stage(9);
    VM_BAR(8);
    if (threadIdx.x == 512) __hip_atomic_fetch_add((GAS unsigned*)(flags + orb * 16), 1u, __ATOMIC_RELAXED, __HIP_MEMORY_SCOPE_AGENT);
    for (int k = 3; k < NS / 2 - 1; ++k) {
      stage(2 * k + 4); stage(2 * k + 5);
      if (k == 4 && sw == 3) {
        if (lane == 0) wait_ge(flags + mt * 16, 32u);
        asm volatile("s_waitcnt vmcnt(0)" ::: "memory");
      }
      if (2 * k + 4 < NS) VM_BAR(8); else VM_BAR(0);
    }
    BAR();
    if (sw == 0) {
      if (lane == 0) __hip_atomic_store((GAS unsigned*)(flags + (8 + tile * 4 + kq) * 16), 1u, __ATOMIC_RELAXED, __HIP_MEMORY_SCOPE_AGENT);
      if (lane < 4 && lane != kq) wait_ge(flags + (8 + tile * 4 + lane) * 16, 1u);
      asm volatile("s_waitcnt vmcnt(0)" ::: "memory");
      BAR();
    }
    asm volatile("k2_code_mark2:");
    return;
  }

  auto compute = [&](auto grp_) {
  constexpr int GRP = decltype(grp_)::value;
  ST(unsigned long long wsum = 0, r0 = rt_(), t0 = mt_();)
  const int q = (lane & 15) >> 2, p = lane & 3;
  const auto sa = __builtin_amdgcn_make_buffer_rsrc((void*)(ws + WS_AP), 0, 144u * 131072u, 0x00020000);
  const unsigned va = lane * 16;
  const unsigned abase = (mt * 16 + 2 * w) * 1024;
  bf16x8 ar[4][2];
  { const unsigned a0 = abase + tix(0) * 262144, a1 = abase + tix(1) * 262144;
    lda(ar[0][0], sa, va, a0); lda(ar[0][1], sa, va, a0 + 1024);
    lda(ar[1][0], sa, va, a0 + 131072); lda(ar[1][1], sa, va, a0 + 131072 + 1024);
    lda(ar[2][0], sa, va, a1); lda(ar[2][1], sa, va, a1 + 1024); }
  const float gsc0 = gs[mt * 256 + w * 32 + (lane & 15)], gsc1 = gs[mt * 256 + w * 32 + 16 + (lane & 15)];
  {
    const char* kp = ws + WS_KVP + (size_t)(oc * 8 + w) * 4096 + lane * 16;
    u32x4 kvl[4];
#pragma unroll
    for (int r = 0; r < 4; ++r) kvl[r] = *(const u32x4*)(kp + r * 1024);
    const char* zp = ws + WS_ZP + ((size_t)oc * 8 * 16 + 2 * g * 16 + (lane & 15)) * 16;
    f32x4 zs = *(const f32x4*)zp + *(const f32x4*)(zp + 256);
    f32x4 kvs = {0.f, 0.f, 0.f, 0.f};
#pragma unroll
    for (int r = 0; r < 4; ++r) kvs += unpk_lo(kvl[r]) + unpk_hi(kvl[r]);
    *(LAS u32x2*)(lb + K2_KV + ((w & 1) * 2 + (w >> 2)) * 1024 + lane * 16 + ((w >> 1) & 1) * 8) = (u32x2){pk2(kvs[0], kvs[1]), pk2(kvs[2], kvs[3])};
    float z0 = zs[0], z1 = zs[1], z2 = zs[2], z3 = zs[3]; XRED2(z0, z1, fadd_); XRED2(z2, z3, fadd_);
    if (w == 0) *(LAS u32x2*)(lb + K2_KV + 4096 + ((lane >> 3) & 1) * 64 + (lane & 3) * 16 + ((lane >> 2) & 1) * 8) = (u32x2){pk2(z0, z1), pk2(z2, z3)};
  }
  f32x4 acc[2][8];
#pragma unroll
  for (int m = 0; m < 2; ++m)
#pragma unroll
    for (int n = 0; n < 8; ++n) acc[m][n] = (f32x4){0.f, 0.f, 0.f, 0.f};
  const unsigned bb0 = lb + (4 * g + q) * 256 + (((4 * (g & 1) + q) << 1 | (p >> 1)) << 4) + (p & 1) * 8;
  unsigned ba[8];
#pragma unroll
  for (int n = 0; n < 8; ++n) ba[n] = bb0 ^ (n << 5);
  unsigned sp2 = 0;
  bf16x8 bw[8];
  auto ldB = [&](int par, int u) -> bf16x8 { const unsigned a = ba[u & 7] + par * K2_SLOT + (u >> 3) * 8192; return trrd2(a, a + 4096); };
  __builtin_amdgcn_s_waitcnt(0xC07F);
  BAR();
  SB(); MFMA_FENCE();
  ST(unsigned long long t1 = mt_();)
#pragma unroll
  for (int u = 0; u < 6; ++u) bw[u] = ldB(0, u);
  auto step = [&](int s, auto par_, auto ia0_, auto ia1_, auto nxt_) {
    constexpr int par = decltype(par_)::value;
    constexpr bool ia0 = decltype(ia0_)::value, ia1 = decltype(ia1_)::value, nxt = decltype(nxt_)::value;
    const unsigned an1 = abase + tix(s + 1) * 262144 + 131072, an2 = abase + tix(s + 2) * 262144;
#pragma unroll
    for (int u = 0; u < 16; ++u) {
      if (u == 10 && nxt && par == 1) {
        BAR();
        const unsigned nx = (sp2 + 32768) & 98304, dx = sp2 ^ nx; sp2 = nx;
#pragma unroll
        for (int n = 0; n < 8; ++n) { ba[n] ^= dx; asm volatile("" : "+v"(ba[n])); }
      }
      if (u < 10) bw[(u + 6) & 7] = ldB(par, u + 6);
      else if (nxt) bw[(u + 6) & 7] = ldB(par ^ 1, u - 10);
      if (ia0 && u < 2) lda(ar[(2 * par + 3) & 3][u], sa, va, an1 + u * 1024);
      if (ia1 && (u == 8 || u == 9)) lda(ar[2 * par][u - 8], sa, va, an2 + (u - 8) * 1024);
      if ((u & 1) == 0 && (u < 10 || nxt)) __builtin_amdgcn_s_waitcnt(0xCA7F);
#pragma unroll
      for (int m = 0; m < 2; ++m) mfma_ip(acc[m][u & 7], bw[u & 7], ar[2 * par + (u >> 3)][m]);
      SB();
    }
  };
#pragma unroll 1
  for (int s = 0; s < NH; s += 2) { step(s, I0{}, I1{}, I1{}, I1{}); step(s + 1, I1{}, I1{}, I1{}, I1{}); }
  {
    MFMA_FENCE(); SB();
    const float s0 = bfr(1.0f - gsc0), s1 = bfr(1.0f - gsc1);
#pragma unroll
    for (int n = 0; n < 8; ++n) { acc[0][n] *= s0; acc[1][n] *= s1; }
    SB(); MFMA_FENCE();
  }
  step(NH, I0{}, I1{}, I1{}, I1{}); step(NH + 1, I1{}, I1{}, I1{}, I1{});
  ST(wsum = mt_();)
  step(NH + 2, I0{}, I1{}, I0{}, I1{});
  step(NH + 3, I1{}, I0{}, I0{}, I0{});
  MFMA_FENCE(); SB();
  ST(unsigned long long t2 = mt_();)
  const auto rp = __builtin_amdgcn_make_buffer_rsrc((void*)(ws + WS_PART), 0, 64u * 262144u, 0x00020000);
  const unsigned ex = lb + 32768;
  if ((w >> 1) == kq) {
#pragma unroll
    for (int m = 0; m < 2; ++m)
#pragma unroll
      for (int n = 0; n < 8; ++n) *(LAS f32x4*)(ex + (((w & 1) * 2 + m) * 8 + n) * 1024 + va) = acc[m][n];
  } else {
#pragma unroll
    for (int m = 0; m < 2; ++m)
#pragma unroll
      for (int n = 0; n < 4; ++n)
        __builtin_amdgcn_raw_buffer_store_b128(__builtin_bit_cast(u32x4, pack8(acc[m][2 * n], acc[m][2 * n + 1])), rp, va + (tile * 4 + kq) * 65536 + w * 8192 + m * 4096 + n * 1024, 0, 16);
  }
  ST(unsigned long long e0 = mt_();)
  WAIT_BAR(0);
  ST(unsigned long long e1 = mt_();)
  f32x4 fin[4];
#pragma unroll
  for (int n = 0; n < 4; ++n) fin[n] = *(LAS f32x4*)(ex + ((w >> 1) * 8 + 4 * (w & 1) + n) * 1024 + va);
  BAR();
  ST(unsigned long long e2 = mt_();)
  {
    u32x4 part[3][2];
#pragma unroll
    for (int j = 0; j < 3; ++j) {
      const int src = (kq + 1 + j) & 3;
      const unsigned po = (tile * 4 + src) * 65536 + (2 * kq + (w >> 2)) * 8192 + ((w >> 1) & 1) * 4096 + 2 * (w & 1) * 1024;
      part[j][0] = __builtin_amdgcn_raw_buffer_load_b128(rp, va, po, 16);
      part[j][1] = __builtin_amdgcn_raw_buffer_load_b128(rp, va, po + 1024, 16);
    }
    uint16_t* orow = out + (size_t)(mt * 256 + kq * 64 + (w >> 1) * 16 + (lane & 15)) * DM + nt128 * 128 + (w & 1) * 64 + 4 * g;
#pragma unroll
    for (int n = 0; n < 2; ++n) {
      const f32x4 v0 = fin[2 * n] + (unpk_lo(part[0][n]) + unpk_lo(part[1][n]) + unpk_lo(part[2][n]));
      const f32x4 v1 = fin[2 * n + 1] + (unpk_hi(part[0][n]) + unpk_hi(part[1][n]) + unpk_hi(part[2][n]));
      *(u32x2*)(orow + n * 32) = (u32x2){pk2(v0[0], v0[1]), pk2(v0[2], v0[3])};
      *(u32x2*)(orow + n * 32 + 16) = (u32x2){pk2(v1[0], v1[1]), pk2(v1[2], v1[3])};
    }
  }
  };
  compute(I0{});
  asm volatile("k2_code_mark:");
}

extern "C" void kernel_launch(void* const* d_in, const int* in_sizes, int n_in, void* d_out, int out_size, void* d_ws, size_t ws_size,
                              hipStream_t stream) {
  const uint16_t* x = (const uint16_t*)d_in[0];
  const uint16_t* wqkv = (const uint16_t*)d_in[1];
  const uint16_t* wphi = (const uint16_t*)d_in[2];
  const uint16_t* wo = (const uint16_t*)d_in[3];
  const uint16_t* wg = (const uint16_t*)d_in[5];
  const uint16_t* wu = (const uint16_t*)d_in[6];
  const uint16_t* wd = (const uint16_t*)d_in[7];
  const uint16_t* gw = (const uint16_t*)d_in[8];
  (void)hipFuncSetAttribute((const void*)k1_kernel, hipFuncAttributeMaxDynamicSharedMemorySize, K1_LDS);
  (void)hipFuncSetAttribute((const void*)k2_kernel, hipFuncAttributeMaxDynamicSharedMemorySize, K2_LDS);
  k1_kernel<<<256, 512, K1_LDS, stream>>>(x, wqkv, wphi, wg, wu, gw, wd, wo, (char*)d_ws);
  k2_kernel<<<256, 768, K2_LDS, stream>>>(wd, wo, (char*)d_ws, (uint16_t*)d_out);
}
```

```cpp
#include <hip/hip_runtime.h>
#include <hip/hip_bf16.h>
#include <stdint.h>
#include <type_traits>
#pragma clang diagnostic ignored "-Wint-to-pointer-cast"

typedef short bf16x8 __attribute__((ext_vector_type(8)));
typedef short s16x4 __attribute__((ext_vector_type(4)));
typedef float f32x4 __attribute__((ext_vector_type(4)));
typedef float f32x2 __attribute__((ext_vector_type(2)));
typedef unsigned u32x4 __attribute__((ext_vector_type(4)));
typedef unsigned u32x2 __attribute__((ext_vector_type(2)));
typedef __bf16 bf16x2_t __attribute__((ext_vector_type(2)));
#define LAS __attribute__((address_space(3)))
#define GAS __attribute__((address_space(1)))
#define DI __device__ __forceinline__

constexpr int SEQ = 2048, DM = 1024, DF = 3584;
constexpr size_t WS_FLAGS = 0;
constexpr size_t WS_G     = 32768;
constexpr size_t WS_ZP    = 65536;
constexpr size_t WS_KVP   = 262144;
constexpr size_t WS_WPF   = 3u << 20;
constexpr size_t WS_QF    = 4u << 20;
constexpr size_t WS_AP    = 16u << 20;
constexpr size_t WS_PART  = 40u << 20;

#define ABL 0
DI f32x4 mfma16(bf16x8 a, bf16x8 b, f32x4 c) { return __builtin_amdgcn_mfma_f32_16x16x32_bf16(a, b, c, 0, 0, 0); }
DI void mfma_ip(f32x4& c, bf16x8 a, bf16x8 b) { asm volatile("v_mfma_f32_16x16x32_bf16 %0, %1, %2, %0" : "+v"(c) : "v"(a), "v"(b)); }
#define MFMA_FENCE() asm volatile("s_nop 7\n\ts_nop 7" ::: "memory")
DI s16x4 trrd(unsigned a) { return __builtin_amdgcn_ds_read_tr16_b64_v4i16((LAS s16x4*)a); }
DI bf16x8 trrd2(unsigned a0, unsigned a1) { s16x4 lo = trrd(a0), hi = trrd(a1); return __builtin_shufflevector(lo, hi, 0, 1, 2, 3, 4, 5, 6, 7); }
DI bf16x8 ldsrd(unsigned a) { return *(LAS bf16x8*)a; }
DI unsigned pk2(float lo, float hi) { f32x2 v = {lo, hi}; bf16x2_t b = __builtin_convertvector(v, bf16x2_t); return __builtin_bit_cast(unsigned, b); }
DI bf16x8 pack8(f32x4 a, f32x4 b) { u32x4 r = {pk2(a[0], a[1]), pk2(a[2], a[3]), pk2(b[0], b[1]), pk2(b[2], b[3])}; return __builtin_bit_cast(bf16x8, r); }
DI f32x4 unpk_lo(u32x4 v) { return (f32x4){__uint_as_float(v[0] << 16), __uint_as_float(v[0] & 0xffff0000u), __uint_as_float(v[1] << 16), __uint_as_float(v[1] & 0xffff0000u)}; }
DI f32x4 unpk_hi(u32x4 v) { return (f32x4){__uint_as_float(v[2] << 16), __uint_as_float(v[2] & 0xffff0000u), __uint_as_float(v[3] << 16), __uint_as_float(v[3] & 0xffff0000u)}; }
DI float bfr(float x) { unsigned u = pk2(x, x); return __uint_as_float(u << 16); }
DI u32x4 mk_srd(const void* p, unsigned bytes) {
  uint64_t a = (uint64_t)p;
  u32x4 r;
  r[0] = __builtin_amdgcn_readfirstlane((unsigned)a);
  r[1] = __builtin_amdgcn_readfirstlane((unsigned)(a >> 32) & 0xffffu);
  r[2] = __builtin_amdgcn_readfirstlane(bytes);
  r[3] = 0x00020000u;
  return r;
}
DI void pfetch(u32x4 srd, unsigned lds, unsigned voff, unsigned soff) {
  asm volatile("s_mov_b32 m0, %0\n\ts_nop 0\n\tbuffer_load_dword %1, %2, %3 offen lds" ::"s"(lds), "v"(voff), "s"(srd), "s"(soff) : "memory");
}
DI void dma16(u32x4 srd, unsigned lds, unsigned voff, unsigned soff) {
  asm volatile("s_mov_b32 m0, %0\n\ts_nop 0\n\tbuffer_load_dwordx4 %1, %2, %3 offen lds" ::"s"(lds), "v"(voff), "s"(srd), "s"(soff) : "memory");
}
#define WAIT_BAR(N) do { asm volatile("" ::: "memory"); __builtin_amdgcn_s_waitcnt(((N) & 15) | 0x70 | (((N) >> 4) << 14)); __builtin_amdgcn_s_barrier(); asm volatile("" ::: "memory"); } while (0)
#define LGKM_BAR() do { asm volatile("" ::: "memory"); __builtin_amdgcn_s_waitcnt(0xC07F); __builtin_amdgcn_s_barrier(); asm volatile("" ::: "memory"); } while (0)
#define SB() __builtin_amdgcn_sched_barrier(0)
#define ST(...)
#define WAIT_BAR_T(N) WAIT_BAR(N)

DI float fmx(float a, float b) { return __builtin_amdgcn_fmed3f(a, b, __builtin_inff()); }
#define XRED2(a, b, OP) do { \
    auto r1_ = __builtin_amdgcn_permlane16_swap(__float_as_uint(a), __float_as_uint(b), false, false); \
    float m_ = OP(__uint_as_float(r1_[0]), __uint_as_float(r1_[1])); \
    auto r2_ = __builtin_amdgcn_permlane32_swap(__float_as_uint(m_), __float_as_uint(m_), false, false); \
    m_ = OP(__uint_as_float(r2_[0]), __uint_as_float(r2_[1])); \
    auto r3_ = __builtin_amdgcn_permlane16_swap(__float_as_uint(m_), __float_as_uint(m_), false, false); \
    a = __uint_as_float(r3_[0]); b = __uint_as_float(r3_[1]); } while (0)
DI float fadd_(float a, float b) { return a + b; }
DI f32x4 exp2v(f32x4 v) { return (f32x4){__builtin_amdgcn_exp2f(v[0]), __builtin_amdgcn_exp2f(v[1]), __builtin_amdgcn_exp2f(v[2]), __builtin_amdgcn_exp2f(v[3])}; }

DI void featmap(bf16x8 fr, bf16x8 wp0, bf16x8 wp1, f32x4& pp0, f32x4& pp1, f32x4& pn0, f32x4& pn1) {
  const f32x4 z4 = {0.f, 0.f, 0.f, 0.f};
  constexpr float L2E = 1.4426950408889634f;
  const f32x4 t0 = mfma16(wp0, fr, z4) * L2E, t1 = mfma16(wp1, fr, z4) * L2E;
  float mx = fmaxf(fmaxf(fmaxf(t0[0], t0[1]), fmaxf(t0[2], t0[3])), fmaxf(fmaxf(t1[0], t1[1]), fmaxf(t1[2], t1[3])));
  float nm = -fminf(fminf(fminf(t0[0], t0[1]), fminf(t0[2], t0[3])), fminf(fminf(t1[0], t1[1]), fminf(t1[2], t1[3])));
  XRED2(mx, nm, fmx);
  pp0 = exp2v(t0 - mx); pp1 = exp2v(t1 - mx);
  pn0 = exp2v(-nm - t0); pn1 = exp2v(-nm - t1);
  const f32x4 s4p = pp0 + pp1, s4n = pn0 + pn1;
  float sp = (s4p[0] + s4p[1]) + (s4p[2] + s4p[3]), sn = (s4n[0] + s4n[1]) + (s4n[2] + s4n[3]);
  XRED2(sp, sn, fadd_);
  const float ip = __builtin_amdgcn_rcpf(sp), in = __builtin_amdgcn_rcpf(sn);
  pp0 *= ip; pp1 *= ip; pn0 *= in; pn1 *= in;
}

DI f32x4 geglu4(f32x4 g, f32x4 u) {
  constexpr float C1 = -2.0f * 0.7978845608028654f * 1.4426950408889634f, C2 = C1 * 0.044715f;
  const f32x4 d = exp2v(g * (g * g * C2 + C1)) + 1.0f;
  const f32x4 r = {__builtin_amdgcn_rcpf(d[0]), __builtin_amdgcn_rcpf(d[1]), __builtin_amdgcn_rcpf(d[2]), __builtin_amdgcn_rcpf(d[3])};
  return (g * u) * r;
}
#define ST_AUX 16
DI void prio_if(int hi) { asm volatile("s_setprio 0\n\ts_cmp_eq_u32 %0, 0\n\ts_cbranch_scc1 1f\n\ts_setprio 1\n1:" ::"s"(hi) : "scc"); }
#define PRIO2 0
#define PRIO1 0
using I0 = std::integral_constant<int, 0>; using I1 = std::integral_constant<int, 1>;

#define K1STG(x) x
constexpr unsigned K1_X0 = 0, K1_XS = K1STG(32768), K1_W0 = 65536, K1_WS = K1STG(40960), K1_WG = 12288, K1_WU = 26624, K1_PHI = 147456, K1_PF = 149504, K1_LDS = 151552 + 1024;

#define WR 5
#define PFU 4
struct K1Dma {
  u32x4 sa, sb, sc;
  unsigned co[4], cc[2];
  unsigned lo[10];
  unsigned stab, stc;
};

template <int WN>
DI void k1_body(unsigned long long& wsum, unsigned long long& tfirst, f32x4 (&acc)[4][10], const unsigned lb, const int wm, const int lane,
                const u32x4 sx, const unsigned v0, const unsigned v1, const unsigned v2, const unsigned vxa, const unsigned vxb, const K1Dma& D) {
  const int g = lane >> 4, q = (lane & 15) >> 2, p = lane & 3, fr = lane & 15;
  const unsigned xrow = wm * 64 + fr;
  const unsigned xb0 = lb + K1_X0 + xrow * 128 + ((g ^ ((xrow >> 1) & 7)) << 4);
  const unsigned xb1 = xb0 ^ 64;
  const unsigned qb0 = lb + K1_W0 + (8 * g + q) * 192 + ((0 ^ (g & 1)) << 5) + p * 8;
  const unsigned qb1 = lb + K1_W0 + (8 * g + q) * 192 + ((1 ^ (g & 1)) << 5) + p * 8;
  const unsigned gb0 = lb + K1_W0 + K1_WG + (8 * g + 4 * (0 ^ (g & 1)) + q) * 224 + p * 8;
  const unsigned gb1 = lb + K1_W0 + K1_WG + (8 * g + 4 * (1 ^ (g & 1)) + q) * 224 + p * 8;
  const unsigned xl = lb + K1_X0 + wm * 8192, wl = lb + K1_W0;
  unsigned sxo = 128, tab = D.stab, tc = D.stc;
  int pt = 3;

  bf16x8 xf[2][4], wf[WR];
  auto ldW = [&](int st, int u) -> bf16x8 {
    const int ks = u / 10, i = u % 10;
    const unsigned so = st * K1_WS;
    constexpr int NQ = WN == 0 ? 4 : 2;
    constexpr int NG = WN == 0 ? 3 : 4;
    constexpr int P0 = WN == 0 ? 4 : 0;
    if (i < NQ) {
      const int ptl = WN == 0 ? 1 + (i >> 1) : 0, nh = i & 1;
      const unsigned b = (nh ? qb1 : qb0) + so + ks * 32 * 192 + ptl * 64;
      return trrd2(b, b + 4 * 192);
    } else {
      const int j = i - NQ, up = j / NG, P = P0 + j % NG;
      const unsigned o = so + ks * 32 * 224 + up * (K1_WU - K1_WG) + P * 32;
      return trrd2(gb0 + o, gb1 + o);
    }
  };
  auto ldX = [&](int st, int ks, int mt) -> bf16x8 { return ldsrd((ks ? xb1 : xb0) + st * K1_XS + mt * 2048); };
  auto mma = [&](int st, int u) {
    const int ks = u / 10, i = u % 10, ri = (st * 20 + u) % WR;
#pragma unroll
    for (int mt = 0; mt < 4; ++mt) {
      if (WN == 0 && (i == 2 || i == 3)) mfma_ip(acc[mt][i], xf[ks][mt], wf[ri]);
      else mfma_ip(acc[mt][i], wf[ri], xf[ks][mt]);
    }
  };
  auto dmaX = [&](int st, int j) { if (ABL != 10) dma16(sx, xl + st * K1_XS + j * 1024, (j & 1) ? vxb : vxa, sxo + j * 16384); };
  auto dmaW = [&](int st, int s) {
    if (ABL == 10) return;
    if (s < 8) dma16((s & 2) ? D.sb : D.sa, wl + st * K1_WS + D.lo[s], s < 4 ? v0 : v1, tab + D.co[s & 3]);
    else dma16(D.sc, wl + st * K1_WS + D.lo[s], v2, tc + D.cc[s - 8]);
  };
  auto warm = [&](int k) {   if (ABL == 10) return;
    const unsigned i0 = (unsigned)(pt - (int)D.co[2 * k]), i = k ? (i0 * D.cc[0]) >> 7 : i0;
    const bool on = i <= D.co[2 * k + 1] && (!k || i * D.cc[1] == i0);
    u32x4 r = k ? D.sb : D.sa;
    r[2] = on ? r[2] : 0u;
    pfetch(r, lb + K1_PF + wm * 512 + k * 256, k ? v1 : v0, (on ? i : 0u) * (k ? D.stc : D.stab));
  };

  constexpr int WD = WR - 1, UB = 20 - WD;
  if (WN == 0) {
#pragma unroll
    for (int sl = 0; sl < 19 - UB; ++sl) {
      if (sl < 5) { dmaW(1, 2 * sl); dmaW(1, 2 * sl + 1); } else { dmaX(1, 2 * (sl - 5)); dmaX(1, 2 * (sl - 5) + 1); }
    }
    WAIT_BAR(ABL == 10 || ABL == 1 || ABL == 5 ? 0 : 2 * (19 - UB));
  } else WAIT_BAR(ABL == 4 || ABL == 5 ? 0 : 4);
  ST(tfirst = mt_();)
#pragma unroll
  for (int mt = 0; mt < 4; ++mt) xf[0][mt] = ldX(0, 0, mt);
#pragma unroll
  for (int u = 0; u < WD; ++u) wf[u] = ldW(0, u);
  SB(); MFMA_FENCE();

  auto step = [&](auto st_, auto dox_, auto dow_) {
    constexpr int st = decltype(st_)::value;
    constexpr bool dox = decltype(dox_)::value, dow = decltype(dow_)::value;
#pragma unroll
    for (int u = 0; u < 20; ++u) {
      if (u == UB + 1) {
        if (WN == 0) { sxo += 128; tab += D.stab; tc += D.stc; } else ++pt;
        if (WN == 0) WAIT_BAR_T(0); else WAIT_BAR_T(63);
        if (dox) wf[(st * 20 + UB + WD) % WR] = ldW(st ^ 1, 0);
      }
      if (u < UB) wf[(st * 20 + u + WD) % WR] = ldW(st, u + WD);
      else if (u > UB && dox) wf[(st * 20 + u + WD) % WR] = ldW(st ^ 1, u - UB);
      if (u >= 2 && u < 6) xf[1][u - 2] = ldX(st, 1, u - 2);
      if (u >= 16 && u > UB && dox) { if (UB >= 16) { if (u == UB + 1) { for (int mt = 0; mt < 4; ++mt) xf[0][mt] = ldX(st ^ 1, 0, mt); } } else xf[0][u - 16] = ldX(st ^ 1, 0, u - 16); }
      if (WN == 0) {
        const int sl = u > UB ? u - UB - 1 : u + 19 - UB;
        const bool post = u > UB;
        if (sl < 5) { if (post ? dow : dox) { dmaW(post ? st : st ^ 1, 2 * sl); dmaW(post ? st : st ^ 1, 2 * sl + 1); } }
        else if (sl < 9) { if (post ? dow : dox) { dmaX(post ? st : st ^ 1, 2 * (sl - 5)); dmaX(post ? st : st ^ 1, 2 * (sl - 5) + 1); } }
      }
      if (WN == 1 && dow && (u == PFU || u == PFU + 2)) warm(u != PFU);
      mma(st, u);
      SB();
    }
  };
  for (int t2 = 0; t2 < 7; ++t2) { step(I0{}, I1{}, I1{}); step(I1{}, I1{}, I1{}); }
  step(I0{}, I1{}, I0{});
  step(I1{}, I0{}, I0{});
  MFMA_FENCE(); SB();
}

__global__ __launch_bounds__(512, 2) void k1_kernel(const uint16_t* __restrict__ x, const uint16_t* __restrict__ wqkv, const uint16_t* __restrict__ wphi,
                                                    const uint16_t* __restrict__ wg, const uint16_t* __restrict__ wu, const uint16_t* __restrict__ gatew,
                                                    const uint16_t* __restrict__ wdown, const uint16_t* __restrict__ wo, char* __restrict__ ws) {
  extern __shared__ __attribute__((aligned(16))) char smem[];
  const unsigned lb = (unsigned)(size_t)(LAS char*)smem;
  unsigned long long wsum = 0; ST(unsigned long long r0 = rt_(); unsigned long long t0 = mt_(); unsigned long long t2 = 0;)
  const int tid = threadIdx.x, lane = tid & 63;
  const int w = __builtin_amdgcn_readfirstlane(tid >> 6);
  const int wn = w >> 2, wm = w & 3;
  const int b = blockIdx.x, xj = b & 7, bi = b >> 3;
  const int c = 4 * xj + (bi & 3), rb = bi >> 2;
  const u32x4 sx = mk_srd(x, SEQ * DM * 2);
  const u32x4 sq = mk_srd(wqkv, DM * 3072 * 2), sg = mk_srd(wg, DM * DF * 2), su = mk_srd(wu, DM * DF * 2);
  f32x4 acc[4][10];
#pragma unroll
  for (int m = 0; m < 4; ++m)
#pragma unroll
    for (int i = 0; i < 10; ++i) acc[m][i] = (f32x4){0.f, 0.f, 0.f, 0.f};
  K1Dma D;
  const int m16 = rb * 16 + wm * 4;
  const auto rw = __builtin_amdgcn_make_buffer_rsrc((void*)ws, 0, 64u << 20, 0x00020000);
#define EPI_LANE() int lane = __builtin_amdgcn_mbcnt_hi(~0u, __builtin_amdgcn_mbcnt_lo(~0u, 0u)); asm volatile("" : "+v"(lane)); const int g = lane >> 4;
#define ST16(val, so) __builtin_amdgcn_raw_buffer_store_b128(__builtin_bit_cast(u32x4, val), rw, lane * 16 + (unsigned)(so), 0, ST_AUX)
  unsigned long long t1 = 0;
  if (wn == 0) {
    const unsigned vxa = (rb * 256 + 64 * wm + (lane >> 3)) * 2048 + (((lane & 7) ^ (lane >> 4)) << 4), vxb = vxa ^ 64;
    const unsigned gtbl = (c & 1) ? 0x0216543u : 0x6543210u;
    auto pat = [&](bool isq, int pi) -> unsigned {
      const int ci = 64 * pi + lane, r = (ci * (isq ? 5462 : 4682)) >> 16, cc = ci - (isq ? 12 : 14) * r;
      const int o8 = (r >> 3) & 1;
      const unsigned vq = r * 6144 + ((cc >> 2) * 1024 + 32 * c + (((cc >> 1) & 1) ^ o8) * 16 + (cc & 1) * 8) * 2;
      const unsigned vg = (r ^ (o8 << 2)) * 7168 + (112 * c + 16 * ((gtbl >> (4 * (cc >> 1))) & 7) + 8 * (cc & 1)) * 2;
      return isq ? vq : vg;
    };
    const bool q01 = wm == 3, q2 = wm >= 2;
    const unsigned v0 = pat(q01, q01 ? 0 : 2 * wm), v1 = pat(q01, q01 ? 1 : 2 * wm + 1), v2 = pat(q2, q2 ? 2 : 6);
#pragma unroll
    for (int i = 0; i < 4; ++i) { D.sa[i] = q01 ? sq[i] : sg[i]; D.sb[i] = q01 ? sq[i] : su[i]; D.sc[i] = q2 ? sq[i] : (wm ? su[i] : sg[i]); }
    D.stab = q01 ? 64 * 6144 : 64 * 7168; D.stc = q2 ? 64 * 6144 : 64 * 7168;
    D.co[0] = 0; D.co[1] = q01 ? 16 * 6144 : 32 * 7168; D.co[2] = q01 ? 32 * 6144 : 0; D.co[3] = q01 ? 48 * 6144 : 32 * 7168;
#pragma unroll
    for (int h = 0; h < 2; ++h)
#pragma unroll
      for (int i = 0; i < 4; ++i) D.lo[4 * h + i] = q01 ? h * 1024 + i * 3072 : K1_WG + (2 * wm + h) * 1024 + i * 7168;
    D.cc[0] = q2 ? (wm - 2) * 32 * 6144 : 0; D.cc[1] = D.cc[0] + (q2 ? 16 * 6144 : 32 * 7168);
    D.lo[8] = q2 ? 2048 + (wm - 2) * 6144 : K1_WG + wm * 14336 + 6144; D.lo[9] = D.lo[8] + (q2 ? 3072 : 7168);
#pragma unroll
    for (int s = 0; s < 10; ++s) {
      if (s < 8) dma16((s & 2) ? D.sb : D.sa, lb + K1_W0 + D.lo[s], s < 4 ? v0 : v1, D.co[s & 3]);
      else dma16(D.sc, lb + K1_W0 + D.lo[s], v2, D.cc[s - 8]);
    }
    if (wm < 2) dma16(mk_srd(wphi, 32 * 32 * 32 * 2), lb + K1_PHI + wm * 1024, lane * 16, c * 2048 + wm * 1024);
    k1_body<0>(wsum, t1, acc, lb, wm, lane, sx, v0, v1, v2, vxa, vxb, D);
    ST(t2 = mt_();)
    EPI_LANE();
    const int q = (lane & 15) >> 2, p = lane & 3;
    const unsigned wpb = lb + K1_PHI + (4 * g + q) * 64 + p * 8;
    const bf16x8 wp0 = trrd2(wpb, wpb + 16 * 64), wp1 = trrd2(wpb + 32, wpb + 16 * 64 + 32);
    if (rb == 0 && wm == 0) { ST16(wp0, WS_WPF + c * 2048); ST16(wp1, WS_WPF + c * 2048 + 1024); }
    const unsigned pkb = lb + wm * 8704;
    const unsigned pw = pkb + (lane & 15) * 136 + g * 8;
#pragma unroll
    for (int mt = 0; mt < 4; ++mt) {
      f32x4 pp0, pp1, pn0, pn1;
      featmap(pack8(acc[mt][0], acc[mt][1]), wp0, wp1, pp0, pp1, pn0, pn1);
      *(LAS u32x2*)(pw + mt * 16 * 136 + 0)  = (u32x2){pk2(pp0[0], pp0[1]), pk2(pp0[2], pp0[3])};
      *(LAS u32x2*)(pw + mt * 16 * 136 + 32) = (u32x2){pk2(pp1[0], pp1[1]), pk2(pp1[2], pp1[3])};
      *(LAS u32x2*)(pw + mt * 16 * 136 + 64) = (u32x2){pk2(pn0[0], pn0[1]), pk2(pn0[2], pn0[3])};
      *(LAS u32x2*)(pw + mt * 16 * 136 + 96) = (u32x2){pk2(pn1[0], pn1[1]), pk2(pn1[2], pn1[3])};
    }
    asm volatile("s_waitcnt lgkmcnt(0)" ::: "memory");
    f32x4 kv[4][2], zz[4];
#pragma unroll
    for (int ft = 0; ft < 4; ++ft) { kv[ft][0] = (f32x4){0.f, 0.f, 0.f, 0.f}; kv[ft][1] = kv[ft][0]; zz[ft] = kv[ft][0]; }
    const bf16x8 ones = {0x3f80, 0x3f80, 0x3f80, 0x3f80, 0x3f80, 0x3f80, 0x3f80, 0x3f80};
    const unsigned pr = pkb + (4 * g + q) * 136 + p * 8;
#pragma unroll
    for (int mp = 0; mp < 2; ++mp) {
      const bf16x8 va = pack8(acc[2 * mp][2], acc[2 * mp + 1][2]), vb = pack8(acc[2 * mp][3], acc[2 * mp + 1][3]);
#pragma unroll
      for (int ft = 0; ft < 4; ++ft) {
        const bf16x8 a = trrd2(pr + (32 * mp) * 136 + ft * 32, pr + (32 * mp + 16) * 136 + ft * 32);
        kv[ft][0] = mfma16(a, va, kv[ft][0]);
        kv[ft][1] = mfma16(a, vb, kv[ft][1]);
        zz[ft] = mfma16(a, ones, zz[ft]);
      }
    }
    const unsigned rd = lb + 65536 + wm * 8192 + lane * 16;
#pragma unroll
    for (int ft = 0; ft < 4; ++ft) {
      *(LAS f32x4*)(rd + (ft * 2 + 0) * 1024) = kv[ft][0];
      *(LAS f32x4*)(rd + (ft * 2 + 1) * 1024) = kv[ft][1];
    }
    { const int ft = lane & 3;
      const f32x4 zsel = ft == 0 ? zz[0] : (ft == 1 ? zz[1] : (ft == 2 ? zz[2] : zz[3]));
      *(LAS f32x4*)(lb + 65536 + 32768 + wm * 256 + ft * 64 + g * 16) = zsel; }
    const int jp = 7 * c + ((c & 1) ? 1 : 4), js = 7 * c + ((c & 1) ? 0 : 6);
    const unsigned hp = WS_AP + ((jp >> 1) * 128 + m16) * 1024, hs = WS_AP + ((js >> 1) * 128 + m16) * 1024 + (js & 1) * 8;
#pragma unroll
    for (int mt = 0; mt < 4; ++mt) {
      const f32x4 h0 = geglu4(acc[mt][4], acc[mt][7]), h1 = geglu4(acc[mt][5], acc[mt][8]), h2 = geglu4(acc[mt][6], acc[mt][9]);
      ST16(pack8(h0, h1), hp + mt * 1024);
      __builtin_amdgcn_raw_buffer_store_b64((u32x2){pk2(h2[0], h2[1]), pk2(h2[2], h2[3])}, rw, lane * 16 + hs + mt * 1024, 0, ST_AUX);
    }
  } else {
    {
      const unsigned vxa = (rb * 256 + 64 * wm + (lane >> 3)) * 2048 + (((lane & 7) ^ (lane >> 4)) << 4), vxb = vxa ^ 64;
#pragma unroll
      for (int j = 0; j < 8; ++j) dma16(sx, lb + K1_X0 + wm * 8192 + j * 1024, (j & 1) ? vxb : vxa, j * 16384);
    }
    const u32x4 sdn = mk_srd(wdown, DF * DM * 2), swo = mk_srd(wo, DM * DM * 2);
    const int r8 = 8 * rb + ((lane >> 2) & 7), l3 = lane & 3;
    const unsigned vx_ = (rb * 256 + (c & 3) * 64 + 32 * wm + (lane >> 1)) * 2048 + (lane & 1) * 64;
    const unsigned vg_ = r8 * 7168 + ((224 * c) & ~63) + l3 * 64;
    const unsigned vq_ = r8 * 6144 + (l3 == 3 ? 0 : l3) * 2048 + 64 * c;
    const unsigned v0 = wm < 2 ? vx_ : (wm == 2 ? vg_ : vq_);
    const unsigned v1 = wm == 2 ? vg_ : (wm < 2 ? (b * 7 + 4 * wm) * 4096 : b * 2 * 4096) + lane * 64;
#pragma unroll
    for (int i = 0; i < 4; ++i) { D.sa[i] = wm < 2 ? sx[i] : (wm == 2 ? sg[i] : sq[i]); D.sb[i] = wm < 2 ? sdn[i] : (wm == 2 ? su[i] : swo[i]); }
    D.stab = wm < 2 ? 128 : (wm == 2 ? 64 * 7168 : 64 * 6144); D.stc = wm == 2 ? 64 * 7168 : 4096;
    D.co[0] = 0; D.co[1] = 15; D.co[2] = wm == 2 ? 0 : (wm == 3 ? 6 : 4 + wm); D.co[3] = wm == 2 ? 15 : (wm == 3 ? 1 : 3 - wm);
    D.cc[0] = wm == 2 ? 128 : 43; D.cc[1] = wm == 2 ? 1 : 3;
    {
      u32x4 sb2 = D.sb; sb2[2] = wm == 2 ? sb2[2] : 0u;
#pragma unroll
      for (int t = 1; t < 3; ++t) { pfetch(D.sa, lb + K1_PF + wm * 512, v0, t * D.stab); pfetch(sb2, lb + K1_PF + wm * 512 + 256, v1, wm == 2 ? t * D.stc : 0u); }
    }
    k1_body<1>(wsum, t1, acc, lb, wm, lane, sx, v0, v1, 0u, 0u, 0u, D);
    ST(t2 = mt_();)
    EPI_LANE();
    const int grow = b * 8 + 2 * wm + (lane >> 5);
    u32x4 gx[4], gw[4];
#pragma unroll
    for (int i = 0; i < 4; ++i) {
      gx[i] = *(const u32x4*)(x + grow * DM + i * 256 + (lane & 31) * 8);
      gw[i] = *(const u32x4*)(gatew + i * 256 + (lane & 31) * 8);
    }
#pragma unroll
    for (int mt = 0; mt < 4; ++mt) ST16(pack8(acc[mt][0], acc[mt][1]), WS_QF + (c * 128 + m16 + mt) * 1024);
    const int j0 = 7 * c + ((c & 1) ? 3 : 0);
    const unsigned hp = WS_AP + ((j0 >> 1) * 128 + m16) * 1024;
#pragma unroll
    for (int mt = 0; mt < 4; ++mt) {
      const f32x4 h0 = geglu4(acc[mt][2], acc[mt][6]), h1 = geglu4(acc[mt][3], acc[mt][7]);
      ST16(pack8(h0, h1), hp + mt * 1024);
      const f32x4 h2 = geglu4(acc[mt][4], acc[mt][8]), h3 = geglu4(acc[mt][5], acc[mt][9]);
      ST16(pack8(h2, h3), hp + 131072 + mt * 1024);
    }
    {
      float s = 0.f;
#pragma unroll
      for (int i = 0; i < 4; ++i)
#pragma unroll
        for (int e = 0; e < 4; ++e) {
          const unsigned pr = pk2(__uint_as_float(gx[i][e] << 16) * __uint_as_float(gw[i][e] << 16), __uint_as_float(gx[i][e] & 0xffff0000u) * __uint_as_float(gw[i][e] & 0xffff0000u));
          s += __uint_as_float(pr << 16) + __uint_as_float(pr & 0xffff0000u);
        }
#pragma unroll
      for (int o = 1; o < 32; o <<= 1) s += __shfl_xor(s, o);
      s = bfr(s);
      const float gv = bfr(1.0f / (1.0f + __expf(-s)));
      if ((lane & 31) == 0) ((float*)(ws + WS_G))[grow] = gv;
      if (wm == 0 && lane == 0) { ((unsigned*)(ws + WS_FLAGS))[(8 + b) * 16] = 0u; if (b < 8) ((unsigned*)(ws + WS_FLAGS))[b * 16] = 0u; }
    }
  }
  LGKM_BAR();
  {
    int tid = w * 64 + __builtin_amdgcn_mbcnt_hi(~0u, __builtin_amdgcn_mbcnt_lo(~0u, 0u)); asm volatile("" : "+v"(tid));
    const unsigned rd = lb + 65536 + tid * 16;
    const f32x4 s = *(LAS f32x4*)(rd) + *(LAS f32x4*)(rd + 8192) + *(LAS f32x4*)(rd + 16384) + *(LAS f32x4*)(rd + 24576);
    __builtin_amdgcn_raw_buffer_store_b64((u32x2){pk2(s[0], s[1]), pk2(s[2], s[3])}, rw, WS_KVP + ((c * 8 + w) * 4 + (rb >> 1)) * 1024 + (tid & 63) * 16 + (rb & 1) * 8, 0, ST_AUX);
    if (tid < 16) {
      const unsigned rz = lb + 65536 + 32768 + tid * 16;
      const f32x4 zs = *(LAS f32x4*)(rz) + *(LAS f32x4*)(rz + 256) + *(LAS f32x4*)(rz + 512) + *(LAS f32x4*)(rz + 768);
      *(f32x4*)(ws + WS_ZP + ((size_t)(c * 8 + rb) * 16 + tid) * 16) = zs;
    }
  }
}

constexpr unsigned K2_SLOT = 16384, K2_KV = 131072, K2_LDS = 131072 + 4608;
constexpr int K2_NPIECE = 10;
constexpr int NS = 18, NH = 14;
#define OUT_AUX 16
#define VM_BAR(N) do { asm volatile("" ::: "memory"); __builtin_amdgcn_s_waitcnt(((N) & 15) | 0xF70 | (((N) >> 4) << 14)); __builtin_amdgcn_s_barrier(); asm volatile("" ::: "memory"); } while (0)
#define BAR() do { asm volatile("" ::: "memory"); __builtin_amdgcn_s_barrier(); asm volatile("" ::: "memory"); } while (0)

DI void wait_ge(const unsigned* flag, unsigned need) {
  for (unsigned spin = 0; spin < (1u << 22); ++spin) {
    if (__hip_atomic_load((const GAS unsigned*)flag, __ATOMIC_RELAXED, __HIP_MEMORY_SCOPE_AGENT) >= need) break;
    __builtin_amdgcn_s_sleep(1);
  }
}
#define lda(d, srd, voff, soff) d = __builtin_bit_cast(bf16x8, __builtin_amdgcn_raw_buffer_load_b128(srd, voff, soff, 16))

__global__ __launch_bounds__(768, 3) void k2_kernel(const uint16_t* __restrict__ wdown, const uint16_t* __restrict__ wo, char* __restrict__ ws,
                                                    uint16_t* __restrict__ out) {
  extern __shared__ __attribute__((aligned(16))) char smem[];
  const unsigned lb = (unsigned)(size_t)(LAS char*)smem;
  const int lane = threadIdx.x & 63;
  const int w = __builtin_amdgcn_readfirstlane(threadIdx.x >> 6);
  const int g = lane >> 4;
  const int b = blockIdx.x, xj = b & 7, bi = b >> 3;
  const int kq = xj >> 1;
  const int mt = 4 * (xj & 1) + (bi >> 3), nt128 = bi & 7;
  const int tile = mt * 8 + nt128;
  const int oc = 4 * xj + (bi & 3), orb = bi >> 2;
  unsigned* const flags = (unsigned*)(ws + WS_FLAGS);
  const float* const gs = (const float*)(ws + WS_G);
  auto tix = [&](int s) -> int { return s < NH ? NH * kq + s : 56 - NH + 4 * kq + s; };

  if (w >= 8) {
    const int sw = w - 8;
    const u32x4 sd = mk_srd(wdown, DF * DM * 2), so_ = mk_srd(wo, DM * DM * 2);
    const unsigned vb0 = (16 * sw + g) * 2048 + nt128 * 256 + (((lane & 15) ^ (g << 1)) << 4), vb1 = (vb0 ^ 128) + 4 * 2048;
    auto stage = [&](int s) {
      if (s < NS) {
        u32x4 r;
#pragma unroll
        for (int j = 0; j < 4; ++j) r[j] = s >= NH ? so_[j] : sd[j];
        const unsigned so = (s >= NH ? 4 * kq + s - NH : NH * kq + s) * 131072, dst = lb + (s & 7) * K2_SLOT + sw * 4096;
#pragma unroll
        for (int i = 0; i < 4; ++i) if (ABL != 12) dma16(r, dst + i * 1024, (i & 1) ? vb1 : vb0, so + (i >> 1) * 16384);
      }
    };
    stage(0); stage(1); stage(2); stage(3);
    bf16x8 qf[4]; float go[4];
    const bf16x8 wp0 = *(const bf16x8*)(ws + WS_WPF + oc * 2048 + lane * 16), wp1 = *(const bf16x8*)(ws + WS_WPF + oc * 2048 + 1024 + lane * 16);
#pragma unroll
    for (int i = 0; i < 4; ++i) {
      qf[i] = *(const bf16x8*)(ws + WS_QF + ((size_t)oc * 128 + orb * 16 + 4 * sw + i) * 1024 + lane * 16);
      go[i] = gs[(orb * 16 + 4 * sw + i) * 16 + (lane & 15)];
    }
    bf16x8 pf0, pf1;
    auto fm = [&](int i) { f32x4 pp0, pp1, pn0, pn1; featmap(qf[i], wp0, wp1, pp0, pp1, pn0, pn1); pf0 = pack8(pp0, pp1); pf1 = pack8(pn0, pn1); };
    VM_BAR(K2_NPIECE + 8);
    stage(4); stage(5);
    bf16x8 ka[2][2], za[2];
#pragma unroll
    for (int ks = 0; ks < 2; ++ks) {
      ka[0][ks] = ldsrd(lb + K2_KV + (0 + ks) * 1024 + lane * 16);
      ka[1][ks] = ldsrd(lb + K2_KV + (2 + ks) * 1024 + lane * 16);
      za[ks] = ldsrd(lb + K2_KV + 4096 + ks * 64 + g * 16);
    }
    const auto rs = __builtin_amdgcn_make_buffer_rsrc((void*)(ws + WS_AP), 0, 144u * 131072u, 0x00020000);
    auto fin = [&](int i) {
      const f32x4 z4 = {0.f, 0.f, 0.f, 0.f};
      f32x4 n0 = mfma16(ka[0][1], pf1, mfma16(ka[0][0], pf0, z4));
      f32x4 n1 = mfma16(ka[1][1], pf1, mfma16(ka[1][0], pf0, z4));
      f32x4 dn = mfma16(za[1], pf1, mfma16(za[0], pf0, z4));
      const float sc = go[i] * __builtin_amdgcn_rcpf(dn[0] + 1e-6f);
      n0 *= sc; n1 *= sc;
      __builtin_amdgcn_raw_buffer_store_b128(__builtin_bit_cast(u32x4, pack8(n0, n1)), rs, lane * 16 + ((112 + oc) * 128 + orb * 16 + 4 * sw + i) * 1024, 0, 16  );
      asm volatile("" ::: "memory");
    };
    fm(0); fin(0); fm(1); fin(1); SB();
    VM_BAR(8);
    stage(6); stage(7);
    fm(2); fin(2); fm(3); fin(3); SB();
    VM_BAR(8);
    stage(8); stage(9);
    VM_BAR(8);
    if (threadIdx.x == 512) __hip_atomic_fetch_add((GAS unsigned*)(flags + orb * 16), 1u, __ATOMIC_RELAXED, __HIP_MEMORY_SCOPE_AGENT);
    for (int k = 3; k < NS / 2 - 1; ++k) {
      stage(2 * k + 4); stage(2 * k + 5);
      if (k == 4 && sw == 3) {
        if (lane == 0) wait_ge(flags + mt * 16, 32u);
        asm volatile("s_waitcnt vmcnt(0)" ::: "memory");
      }
      if (2 * k + 4 < NS) VM_BAR(8); else VM_BAR(0);
    }
    BAR();
    if (sw == 0) {
      if (lane == 0) __hip_atomic_store((GAS unsigned*)(flags + (8 + tile * 4 + kq) * 16), 1u, __ATOMIC_RELAXED, __HIP_MEMORY_SCOPE_AGENT);
      if (lane < 4 && lane != kq) wait_ge(flags + (8 + tile * 4 + lane) * 16, 1u);
      asm volatile("s_waitcnt vmcnt(0)" ::: "memory");
      BAR();
    }
    return;
  }

  auto compute = [&](auto grp_) {
  constexpr int GRP = decltype(grp_)::value;
  ST(unsigned long long wsum = 0, r0 = rt_(), t0 = mt_();)
  const int q = (lane & 15) >> 2, p = lane & 3;
  const auto sa = __builtin_amdgcn_make_buffer_rsrc((void*)(ws + WS_AP), 0, 144u * 131072u, 0x00020000);
  const unsigned va = lane * 16;
  const unsigned abase = (mt * 16 + 2 * w) * 1024;
  bf16x8 ar[4][2];
  { const unsigned a0 = abase + tix(0) * 262144, a1 = abase + tix(1) * 262144;
    lda(ar[0][0], sa, va, a0); lda(ar[0][1], sa, va, a0 + 1024);
    lda(ar[1][0], sa, va, a0 + 131072); lda(ar[1][1], sa, va, a0 + 131072 + 1024);
    lda(ar[2][0], sa, va, a1); lda(ar[2][1], sa, va, a1 + 1024); }
  const float gsc0 = gs[mt * 256 + w * 32 + (lane & 15)], gsc1 = gs[mt * 256 + w * 32 + 16 + (lane & 15)];
  {
    const char* kp = ws + WS_KVP + (size_t)(oc * 8 + w) * 4096 + lane * 16;
    u32x4 kvl[4];
#pragma unroll
    for (int r = 0; r < 4; ++r) kvl[r] = *(const u32x4*)(kp + r * 1024);
    const char* zp = ws + WS_ZP + ((size_t)oc * 8 * 16 + 2 * g * 16 + (lane & 15)) * 16;
    f32x4 zs = *(const f32x4*)zp + *(const f32x4*)(zp + 256);
    f32x4 kvs = {0.f, 0.f, 0.f, 0.f};
#pragma unroll
    for (int r = 0; r < 4; ++r) kvs += unpk_lo(kvl[r]) + unpk_hi(kvl[r]);
    *(LAS u32x2*)(lb + K2_KV + ((w & 1) * 2 + (w >> 2)) * 1024 + lane * 16 + ((w >> 1) & 1) * 8) = (u32x2){pk2(kvs[0], kvs[1]), pk2(kvs[2], kvs[3])};
    float z0 = zs[0], z1 = zs[1], z2 = zs[2], z3 = zs[3]; XRED2(z0, z1, fadd_); XRED2(z2, z3, fadd_);
    if (w == 0) *(LAS u32x2*)(lb + K2_KV + 4096 + ((lane >> 3) & 1) * 64 + (lane & 3) * 16 + ((lane >> 2) & 1) * 8) = (u32x2){pk2(z0, z1), pk2(z2, z3)};
  }
  f32x4 acc[2][8];
#pragma unroll
  for (int m = 0; m < 2; ++m)
#pragma unroll
    for (int n = 0; n < 8; ++n) acc[m][n] = (f32x4){0.f, 0.f, 0.f, 0.f};
  const unsigned bb0 = lb + (4 * g + q) * 256 + (((4 * (g & 1) + q) << 1 | (p >> 1)) << 4) + (p & 1) * 8;
  unsigned ba[8];
#pragma unroll
  for (int n = 0; n < 8; ++n) ba[n] = bb0 ^ (n << 5);
  unsigned sp2 = 0;
  bf16x8 bw[8];
  auto ldB = [&](int par, int u) -> bf16x8 { const unsigned a = ba[u & 7] + par * K2_SLOT + (u >> 3) * 8192; return trrd2(a, a + 4096); };
  __builtin_amdgcn_s_waitcnt(0xC07F);
  BAR();
  SB(); MFMA_FENCE();
  ST(unsigned long long t1 = mt_();)
#pragma unroll
  for (int u = 0; u < 6; ++u) bw[u] = ldB(0, u);
  auto step = [&](int s, auto par_, auto ia0_, auto ia1_, auto nxt_) {
    constexpr int par = decltype(par_)::value;
    constexpr bool ia0 = decltype(ia0_)::value, ia1 = decltype(ia1_)::value, nxt = decltype(nxt_)::value;
    const unsigned an1 = abase + tix(s + 1) * 262144 + 131072, an2 = abase + tix(s + 2) * 262144;
#pragma unroll
    for (int u = 0; u < 16; ++u) {
      if (u == 10 && nxt && par == 1) {
        BAR();
        const unsigned nx = (sp2 + 32768) & 98304, dx = sp2 ^ nx; sp2 = nx;
#pragma unroll
        for (int n = 0; n < 8; ++n) { ba[n] ^= dx; asm volatile("" : "+v"(ba[n])); }
      }
      if (u < 10) bw[(u + 6) & 7] = ldB(par, u + 6);
      else if (nxt) bw[(u + 6) & 7] = ldB(par ^ 1, u - 10);
      if (ia0 && u < 2) lda(ar[(2 * par + 3) & 3][u], sa, va, an1 + u * 1024);
      if (ia1 && (u == 8 || u == 9)) lda(ar[2 * par][u - 8], sa, va, an2 + (u - 8) * 1024);
      if ((u & 1) == 0 && (u < 10 || nxt)) __builtin_amdgcn_s_waitcnt(0xCA7F);
#pragma unroll
      for (int m = 0; m < 2; ++m) mfma_ip(acc[m][u & 7], bw[u & 7], ar[2 * par + (u >> 3)][m]);
      SB();
    }
  };
#pragma unroll 1
  for (int s = 0; s < NH; s += 2) { step(s, I0{}, I1{}, I1{}, I1{}); step(s + 1, I1{}, I1{}, I1{}, I1{}); }
  {
    MFMA_FENCE(); SB();
    const float s0 = bfr(1.0f - gsc0), s1 = bfr(1.0f - gsc1);
#pragma unroll
    for (int n = 0; n < 8; ++n) { acc[0][n] *= s0; acc[1][n] *= s1; }
    SB(); MFMA_FENCE();
  }
  step(NH, I0{}, I1{}, I1{}, I1{}); step(NH + 1, I1{}, I1{}, I1{}, I1{});
  ST(wsum = mt_();)
  step(NH + 2, I0{}, I1{}, I0{}, I1{});
  step(NH + 3, I1{}, I0{}, I0{}, I0{});
  MFMA_FENCE(); SB();
  ST(unsigned long long t2 = mt_();)
  const auto rp = __builtin_amdgcn_make_buffer_rsrc((void*)(ws + WS_PART), 0, 64u * 262144u, 0x00020000);
  const unsigned ex = lb + 32768;
  if ((w >> 1) == kq) {
#pragma unroll
    for (int m = 0; m < 2; ++m)
#pragma unroll
      for (int n = 0; n < 8; ++n) *(LAS f32x4*)(ex + (((w & 1) * 2 + m) * 8 + n) * 1024 + va) = acc[m][n];
  } else {
#pragma unroll
    for (int m = 0; m < 2; ++m)
#pragma unroll
      for (int n = 0; n < 4; ++n)
        __builtin_amdgcn_raw_buffer_store_b128(__builtin_bit_cast(u32x4, pack8(acc[m][2 * n], acc[m][2 * n + 1])), rp, va + (tile * 4 + kq) * 65536 + w * 8192 + m * 4096 + n * 1024, 0, 16);
  }
  ST(unsigned long long e0 = mt_();)
  WAIT_BAR(0);
  ST(unsigned long long e1 = mt_();)
  f32x4 fin[4];
#pragma unroll
  for (int n = 0; n < 4; ++n) fin[n] = *(LAS f32x4*)(ex + ((w >> 1) * 8 + 4 * (w & 1) + n) * 1024 + va);
  BAR();
  ST(unsigned long long e2 = mt_();)
  {
    u32x4 part[3][2];
#pragma unroll
    for (int j = 0; j < 3; ++j) {
      const int src = (kq + 1 + j) & 3;
      const unsigned po = (tile * 4 + src) * 65536 + (2 * kq + (w >> 2)) * 8192 + ((w >> 1) & 1) * 4096 + 2 * (w & 1) * 1024;
      part[j][0] = __builtin_amdgcn_raw_buffer_load_b128(rp, va, po, 16);
      part[j][1] = __builtin_amdgcn_raw_buffer_load_b128(rp, va, po + 1024, 16);
    }
    const auto ro = __builtin_amdgcn_make_buffer_rsrc((void*)out, 0, SEQ * DM * 2, 0x00020000);
    const unsigned oo = ((mt * 256 + kq * 64 + (w >> 1) * 16 + (lane & 15)) * DM + nt128 * 128 + (w & 1) * 64 + (g & 1) * 16 + (g >> 1) * 8) * 2;
#pragma unroll
    for (int n = 0; n < 2; ++n) {
      const f32x4 v0 = fin[2 * n] + (unpk_lo(part[0][n]) + unpk_lo(part[1][n]) + unpk_lo(part[2][n]));
      const f32x4 v1 = fin[2 * n + 1] + (unpk_hi(part[0][n]) + unpk_hi(part[1][n]) + unpk_hi(part[2][n]));
      const auto r0 = __builtin_amdgcn_permlane16_swap(pk2(v0[0], v0[1]), pk2(v1[0], v1[1]), false, false);
      const auto r1 = __builtin_amdgcn_permlane16_swap(pk2(v0[2], v0[3]), pk2(v1[2], v1[3]), false, false);
      __builtin_amdgcn_raw_buffer_store_b128((u32x4){r0[0], r1[0], r0[1], r1[1]}, ro, oo + n * 64, 0, OUT_AUX);
    }
  }
  };
  compute(I0{});
}

extern "C" void kernel_launch(void* const* d_in, const int* in_sizes, int n_in, void* d_out, int out_size, void* d_ws, size_t ws_size,
                              hipStream_t stream) {
  const uint16_t* x = (const uint16_t*)d_in[0];
  const uint16_t* wqkv = (const uint16_t*)d_in[1];
  const uint16_t* wphi = (const uint16_t*)d_in[2];
  const uint16_t* wo = (const uint16_t*)d_in[3];
  const uint16_t* wg = (const uint16_t*)d_in[5];
  const uint16_t* wu = (const uint16_t*)d_in[6];
  const uint16_t* wd = (const uint16_t*)d_in[7];
  const uint16_t* gw = (const uint16_t*)d_in[8];
  (void)hipFuncSetAttribute((const void*)k1_kernel, hipFuncAttributeMaxDynamicSharedMemorySize, K1_LDS);
  (void)hipFuncSetAttribute((const void*)k2_kernel, hipFuncAttributeMaxDynamicSharedMemorySize, K2_LDS);
  k1_kernel<<<256, 512, K1_LDS, stream>>>(x, wqkv, wphi, wg, wu, gw, wd, wo, (char*)d_ws);
  k2_kernel<<<256, 768, K2_LDS, stream>>>(wd, wo, (char*)d_ws, (uint16_t*)d_out);
}
```

```cpp
#include <hip/hip_runtime.h>
#include <hip/hip_bf16.h>
#include <stdint.h>
#include <type_traits>
#pragma clang diagnostic ignored "-Wint-to-pointer-cast"

typedef short bf16x8 __attribute__((ext_vector_type(8)));
typedef short s16x4 __attribute__((ext_vector_type(4)));
typedef float f32x4 __attribute__((ext_vector_type(4)));
typedef float f32x2 __attribute__((ext_vector_type(2)));
typedef unsigned u32x4 __attribute__((ext_vector_type(4)));
typedef unsigned u32x2 __attribute__((ext_vector_type(2)));
typedef __bf16 bf16x2_t __attribute__((ext_vector_type(2)));
#define LAS __attribute__((address_space(3)))
#define GAS __attribute__((address_space(1)))
#define DI __device__ __forceinline__

constexpr int SEQ = 2048, DM = 1024, DF = 3584;
constexpr size_t WS_FLAGS = 0;
constexpr size_t WS_G     = 32768;
constexpr size_t WS_ZP    = 65536;
constexpr size_t WS_KVP   = 262144;
constexpr size_t WS_WPF   = 3u << 20;
constexpr size_t WS_QF    = 4u << 20;
constexpr size_t WS_AP    = 16u << 20;
constexpr size_t WS_PART  = 40u << 20;

#define ABL 0
DI f32x4 mfma16(bf16x8 a, bf16x8 b, f32x4 c) { return __builtin_amdgcn_mfma_f32_16x16x32_bf16(a, b, c, 0, 0, 0); }
DI void mfma_ip(f32x4& c, bf16x8 a, bf16x8 b) { asm volatile("v_mfma_f32_16x16x32_bf16 %0, %1, %2, %0" : "+v"(c) : "v"(a), "v"(b)); }
#define MFMA_FENCE() asm volatile("s_nop 7\n\ts_nop 7" ::: "memory")
DI s16x4 trrd(unsigned a) { return __builtin_amdgcn_ds_read_tr16_b64_v4i16((LAS s16x4*)a); }
DI bf16x8 trrd2(unsigned a0, unsigned a1) { s16x4 lo = trrd(a0), hi = trrd(a1); return __builtin_shufflevector(lo, hi, 0, 1, 2, 3, 4, 5, 6, 7); }
DI bf16x8 ldsrd(unsigned a) { return *(LAS bf16x8*)a; }
DI unsigned pk2(float lo, float hi) { f32x2 v = {lo, hi}; bf16x2_t b = __builtin_convertvector(v, bf16x2_t); return __builtin_bit_cast(unsigned, b); }
DI bf16x8 pack8(f32x4 a, f32x4 b) { u32x4 r = {pk2(a[0], a[1]), pk2(a[2], a[3]), pk2(b[0], b[1]), pk2(b[2], b[3])}; return __builtin_bit_cast(bf16x8, r); }
DI f32x4 unpk_lo(u32x4 v) { return (f32x4){__uint_as_float(v[0] << 16), __uint_as_float(v[0] & 0xffff0000u), __uint_as_float(v[1] << 16), __uint_as_float(v[1] & 0xffff0000u)}; }
DI f32x4 unpk_hi(u32x4 v) { return (f32x4){__uint_as_float(v[2] << 16), __uint_as_float(v[2] & 0xffff0000u), __uint_as_float(v[3] << 16), __uint_as_float(v[3] & 0xffff0000u)}; }
DI float bfr(float x) { unsigned u = pk2(x, x); return __uint_as_float(u << 16); }
DI u32x4 mk_srd(const void* p, unsigned bytes) {
  uint64_t a = (uint64_t)p;
  u32x4 r;
  r[0] = __builtin_amdgcn_readfirstlane((unsigned)a);
  r[1] = __builtin_amdgcn_readfirstlane((unsigned)(a >> 32) & 0xffffu);
  r[2] = __builtin_amdgcn_readfirstlane(bytes);
  r[3] = 0x00020000u;
  return r;
}
DI void pfetch(u32x4 srd, unsigned lds, unsigned voff, unsigned soff) {
  asm volatile("s_mov_b32 m0, %0\n\ts_nop 0\n\tbuffer_load_dword %1, %2, %3 offen lds" ::"s"(lds), "v"(voff), "s"(srd), "s"(soff) : "memory");
}
DI void dma16(u32x4 srd, unsigned lds, unsigned voff, unsigned soff) {
  asm volatile("s_mov_b32 m0, %0\n\ts_nop 0\n\tbuffer_load_dwordx4 %1, %2, %3 offen lds" ::"s"(lds), "v"(voff), "s"(srd), "s"(soff) : "memory");
}
#define WAIT_BAR(N) do { asm volatile("" ::: "memory"); __builtin_amdgcn_s_waitcnt(((N) & 15) | 0x70 | (((N) >> 4) << 14)); __builtin_amdgcn_s_barrier(); asm volatile("" ::: "memory"); } while (0)
#define LGKM_BAR() do { asm volatile("" ::: "memory"); __builtin_amdgcn_s_waitcnt(0xC07F); __builtin_amdgcn_s_barrier(); asm volatile("" ::: "memory"); } while (0)
#define SB() __builtin_amdgcn_sched_barrier(0)
#define ST(...)
#define WAIT_BAR_T(N) WAIT_BAR(N)

DI float fmx(float a, float b) { return __builtin_amdgcn_fmed3f(a, b, __builtin_inff()); }
#define XRED2(a, b, OP) do { \
    auto r1_ = __builtin_amdgcn_permlane16_swap(__float_as_uint(a), __float_as_uint(b), false, false); \
    float m_ = OP(__uint_as_float(r1_[0]), __uint_as_float(r1_[1])); \
    auto r2_ = __builtin_amdgcn_permlane32_swap(__float_as_uint(m_), __float_as_uint(m_), false, false); \
    m_ = OP(__uint_as_float(r2_[0]), __uint_as_float(r2_[1])); \
    auto r3_ = __builtin_amdgcn_permlane16_swap(__float_as_uint(m_), __float_as_uint(m_), false, false); \
    a = __uint_as_float(r3_[0]); b = __uint_as_float(r3_[1]); } while (0)
DI float fadd_(float a, float b) { return a + b; }
DI f32x4 exp2v(f32x4 v) { return (f32x4){__builtin_amdgcn_exp2f(v[0]), __builtin_amdgcn_exp2f(v[1]), __builtin_amdgcn_exp2f(v[2]), __builtin_amdgcn_exp2f(v[3])}; }

DI void featmap(bf16x8 fr, bf16x8 wp0, bf16x8 wp1, f32x4& pp0, f32x4& pp1, f32x4& pn0, f32x4& pn1) {
  const f32x4 z4 = {0.f, 0.f, 0.f, 0.f};
  constexpr float L2E = 1.4426950408889634f;
  const f32x4 t0 = mfma16(wp0, fr, z4) * L2E, t1 = mfma16(wp1, fr, z4) * L2E;
  float mx = fmaxf(fmaxf(fmaxf(t0[0], t0[1]), fmaxf(t0[2], t0[3])), fmaxf(fmaxf(t1[0], t1[1]), fmaxf(t1[2], t1[3])));
  float nm = -fminf(fminf(fminf(t0[0], t0[1]), fminf(t0[2], t0[3])), fminf(fminf(t1[0], t1[1]), fminf(t1[2], t1[3])));
  XRED2(mx, nm, fmx);
  pp0 = exp2v(t0 - mx); pp1 = exp2v(t1 - mx);
  pn0 = exp2v(-nm - t0); pn1 = exp2v(-nm - t1);
  const f32x4 s4p = pp0 + pp1, s4n = pn0 + pn1;
  float sp = (s4p[0] + s4p[1]) + (s4p[2] + s4p[3]), sn = (s4n[0] + s4n[1]) + (s4n[2] + s4n[3]);
  XRED2(sp, sn, fadd_);
  const float ip = __builtin_amdgcn_rcpf(sp), in = __builtin_amdgcn_rcpf(sn);
  pp0 *= ip; pp1 *= ip; pn0 *= in; pn1 *= in;
}

DI f32x4 geglu4(f32x4 g, f32x4 u) {
  constexpr float C1 = -2.0f * 0.7978845608028654f * 1.4426950408889634f, C2 = C1 * 0.044715f;
  const f32x4 d = exp2v(g * (g * g * C2 + C1)) + 1.0f;
  const f32x4 r = {__builtin_amdgcn_rcpf(d[0]), __builtin_amdgcn_rcpf(d[1]), __builtin_amdgcn_rcpf(d[2]), __builtin_amdgcn_rcpf(d[3])};
  return (g * u) * r;
}
#define ST_AUX 16
DI void prio_if(int hi) { asm volatile("s_setprio 0\n\ts_cmp_eq_u32 %0, 0\n\ts_cbranch_scc1 1f\n\ts_setprio 1\n1:" ::"s"(hi) : "scc"); }
#define PRIO2 0
#define PRIO1 0
using I0 = std::integral_constant<int, 0>; using I1 = std::integral_constant<int, 1>;

#define K1STG(x) x
constexpr unsigned K1_X0 = 0, K1_XS = K1STG(32768), K1_W0 = 65536, K1_WS = K1STG(40960), K1_WG = 12288, K1_WU = 26624, K1_PHI = 147456, K1_PF = 149504, K1_LDS = 151552 + 1024;

#define WR 5
#define PFU 4
struct K1Dma {
  u32x4 sa, sb, sc;
  unsigned co[4], cc[2];
  unsigned lo[10];
  unsigned stab, stc;
};

template <int WN>
DI void k1_body(unsigned long long& wsum, unsigned long long& tfirst, f32x4 (&acc)[4][10], const unsigned lb, const int wm, const int lane,
                const u32x4 sx, const unsigned v0, const unsigned v1, const unsigned v2, const unsigned vxa, const unsigned vxb, const K1Dma& D) {
  const int g = lane >> 4, q = (lane & 15) >> 2, p = lane & 3, fr = lane & 15;
  const unsigned xrow = wm * 64 + fr;
  const unsigned xb0 = lb + K1_X0 + xrow * 128 + ((g ^ ((xrow >> 1) & 7)) << 4);
  const unsigned xb1 = xb0 ^ 64;
  const unsigned qb0 = lb + K1_W0 + (8 * g + q) * 192 + ((0 ^ (g & 1)) << 5) + p * 8;
  const unsigned qb1 = lb + K1_W0 + (8 * g + q) * 192 + ((1 ^ (g & 1)) << 5) + p * 8;
  const unsigned gb0 = lb + K1_W0 + K1_WG + (8 * g + 4 * (0 ^ (g & 1)) + q) * 224 + p * 8;
  const unsigned gb1 = lb + K1_W0 + K1_WG + (8 * g + 4 * (1 ^ (g & 1)) + q) * 224 + p * 8;
  const unsigned xl = lb + K1_X0 + wm * 8192, wl = lb + K1_W0;
  unsigned sxo = 128, tab = D.stab, tc = D.stc;
  int pt = 3;

  bf16x8 xf[2][4], wf[WR];
  auto ldW = [&](int st, int u) -> bf16x8 {
    const int ks = u / 10, i = u % 10;
    const unsigned so = st * K1_WS;
    constexpr int NQ = WN == 0 ? 4 : 2;
    constexpr int NG = WN == 0 ? 3 : 4;
    constexpr int P0 = WN == 0 ? 4 : 0;
    if (i < NQ) {
      const int ptl = WN == 0 ? 1 + (i >> 1) : 0, nh = i & 1;
      const unsigned b = (nh ? qb1 : qb0) + so + ks * 32 * 192 + ptl * 64;
      return trrd2(b, b + 4 * 192);
    } else {
      const int j = i - NQ, up = j / NG, P = P0 + j % NG;
      const unsigned o = so + ks * 32 * 224 + up * (K1_WU - K1_WG) + P * 32;
      return trrd2(gb0 + o, gb1 + o);
    }
  };
  auto ldX = [&](int st, int ks, int mt) -> bf16x8 { return ldsrd((ks ? xb1 : xb0) + st * K1_XS + mt * 2048); };
  auto mma = [&](int st, int u) {
    const int ks = u / 10, i = u % 10, ri = (st * 20 + u) % WR;
#pragma unroll
    for (int mt = 0; mt < 4; ++mt) {
      if (WN == 0 && (i == 2 || i == 3)) mfma_ip(acc[mt][i], xf[ks][mt], wf[ri]);
      else mfma_ip(acc[mt][i], wf[ri], xf[ks][mt]);
    }
  };
  auto dmaX = [&](int st, int j) { if (ABL != 10) dma16(sx, xl + st * K1_XS + j * 1024, (j & 1) ? vxb : vxa, sxo + j * 16384); };
  auto dmaW = [&](int st, int s) {
    if (ABL == 10) return;
    if (s < 8) dma16((s & 2) ? D.sb : D.sa, wl + st * K1_WS + D.lo[s], s < 4 ? v0 : v1, tab + D.co[s & 3]);
    else dma16(D.sc, wl + st * K1_WS + D.lo[s], v2, tc + D.cc[s - 8]);
  };
  auto warm = [&](int k) {   if (ABL == 10) return;
    const unsigned i0 = (unsigned)(pt - (int)D.co[2 * k]), i = k ? (i0 * D.cc[0]) >> 7 : i0;
    const bool on = i <= D.co[2 * k + 1] && (!k || i * D.cc[1] == i0);
    u32x4 r = k ? D.sb : D.sa;
    r[2] = on ? r[2] : 0u;
    pfetch(r, lb + K1_PF + wm * 512 + k * 256, k ? v1 : v0, (on ? i : 0u) * (k ? D.stc : D.stab));
  };

  constexpr int WD = WR - 1, UB = 20 - WD;
  if (WN == 0) {
#pragma unroll
    for (int sl = 0; sl < 19 - UB; ++sl) {
      if (sl < 5) { dmaW(1, 2 * sl); dmaW(1, 2 * sl + 1); } else { dmaX(1, 2 * (sl - 5)); dmaX(1, 2 * (sl - 5) + 1); }
    }
    WAIT_BAR(ABL == 10 || ABL == 1 || ABL == 5 ? 0 : 2 * (19 - UB));
  } else WAIT_BAR(ABL == 4 || ABL == 5 ? 0 : 4);
  ST(tfirst = mt_();)
#pragma unroll
  for (int mt = 0; mt < 4; ++mt) xf[0][mt] = ldX(0, 0, mt);
#pragma unroll
  for (int u = 0; u < WD; ++u) wf[u] = ldW(0, u);
  SB(); MFMA_FENCE();

  auto step = [&](auto st_, auto dox_, auto dow_) {
    constexpr int st = decltype(st_)::value;
    constexpr bool dox = decltype(dox_)::value, dow = decltype(dow_)::value;
#pragma unroll
    for (int u = 0; u < 20; ++u) {
      if (u == UB + 1) {
        if (WN == 0) { sxo += 128; tab += D.stab; tc += D.stc; } else ++pt;
        if (WN == 0) WAIT_BAR_T(0); else WAIT_BAR_T(63);
        if (dox) wf[(st * 20 + UB + WD) % WR] = ldW(st ^ 1, 0);
      }
      if (u < UB) wf[(st * 20 + u + WD) % WR] = ldW(st, u + WD);
      else if (u > UB && dox) wf[(st * 20 + u + WD) % WR] = ldW(st ^ 1, u - UB);
      if (u >= 2 && u < 6) xf[1][u - 2] = ldX(st, 1, u - 2);
      if (u >= 16 && u > UB && dox) { if (UB >= 16) { if (u == UB + 1) { for (int mt = 0; mt < 4; ++mt) xf[0][mt] = ldX(st ^ 1, 0, mt); } } else xf[0][u - 16] = ldX(st ^ 1, 0, u - 16); }
      if (WN == 0) {
        const int sl = u > UB ? u - UB - 1 : u + 19 - UB;
        const bool post = u > UB;
        if (sl < 5) { if (post ? dow : dox) { dmaW(post ? st : st ^ 1, 2 * sl); dmaW(post ? st : st ^ 1, 2 * sl + 1); } }
        else if (sl < 9) { if (post ? dow : dox) { dmaX(post ? st : st ^ 1, 2 * (sl - 5)); dmaX(post ? st : st ^ 1, 2 * (sl - 5) + 1); } }
      }
      if (WN == 1 && dow && (u == PFU || u == PFU + 2)) warm(u != PFU);
      mma(st, u);
      SB();
    }
  };
  for (int t2 = 0; t2 < 7; ++t2) { step(I0{}, I1{}, I1{}); step(I1{}, I1{}, I1{}); }
  step(I0{}, I1{}, I0{});
  step(I1{}, I0{}, I0{});
  MFMA_FENCE(); SB();
}

__global__ __launch_bounds__(512, 2) void k1_kernel(const uint16_t* __restrict__ x, const uint16_t* __restrict__ wqkv, const uint16_t* __restrict__ wphi,
                                                    const uint16_t* __restrict__ wg, const uint16_t* __restrict__ wu, const uint16_t* __restrict__ gatew,
                                                    const uint16_t* __restrict__ wdown, const uint16_t* __restrict__ wo, char* __restrict__ ws) {
  extern __shared__ __attribute__((aligned(16))) char smem[];
  const unsigned lb = (unsigned)(size_t)(LAS char*)smem;
  unsigned long long wsum = 0; ST(unsigned long long r0 = rt_(); unsigned long long t0 = mt_(); unsigned long long t2 = 0;)
  const int tid = threadIdx.x, lane = tid & 63;
  const int w = __builtin_amdgcn_readfirstlane(tid >> 6);
  const int wn = w >> 2, wm = w & 3;
  const int b = blockIdx.x, xj = b & 7, bi = b >> 3;
  const int c = 4 * xj + (bi & 3), rb = bi >> 2;
  const u32x4 sx = mk_srd(x, SEQ * DM * 2);
  const u32x4 sq = mk_srd(wqkv, DM * 3072 * 2), sg = mk_srd(wg, DM * DF * 2), su = mk_srd(wu, DM * DF * 2);
  f32x4 acc[4][10];
#pragma unroll
  for (int m = 0; m < 4; ++m)
#pragma unroll
    for (int i = 0; i < 10; ++i) acc[m][i] = (f32x4){0.f, 0.f, 0.f, 0.f};
  K1Dma D;
  const int m16 = rb * 16 + wm * 4;
  const auto rw = __builtin_amdgcn_make_buffer_rsrc((void*)ws, 0, 64u << 20, 0x00020000);
#define EPI_LANE() int lane = __builtin_amdgcn_mbcnt_hi(~0u, __builtin_amdgcn_mbcnt_lo(~0u, 0u)); asm volatile("" : "+v"(lane)); const int g = lane >> 4;
#define ST16(val, so) __builtin_amdgcn_raw_buffer_store_b128(__builtin_bit_cast(u32x4, val), rw, lane * 16 + (unsigned)(so), 0, ST_AUX)
  unsigned long long t1 = 0;
  if (wn == 0) {
    const unsigned vxa = (rb * 256 + 64 * wm + (lane >> 3)) * 2048 + (((lane & 7) ^ (lane >> 4)) << 4), vxb = vxa ^ 64;
    const unsigned gtbl = (c & 1) ? 0x0216543u : 0x6543210u;
    auto pat = [&](bool isq, int pi) -> unsigned {
      const int ci = 64 * pi + lane, r = (ci * (isq ? 5462 : 4682)) >> 16, cc = ci - (isq ? 12 : 14) * r;
      const int o8 = (r >> 3) & 1;
      const unsigned vq = r * 6144 + ((cc >> 2) * 1024 + 32 * c + (((cc >> 1) & 1) ^ o8) * 16 + (cc & 1) * 8) * 2;
      const unsigned vg = (r ^ (o8 << 2)) * 7168 + (112 * c + 16 * ((gtbl >> (4 * (cc >> 1))) & 7) + 8 * (cc & 1)) * 2;
      return isq ? vq : vg;
    };
    const bool q01 = wm == 3, q2 = wm >= 2;
    const unsigned v0 = pat(q01, q01 ? 0 : 2 * wm), v1 = pat(q01, q01 ? 1 : 2 * wm + 1), v2 = pat(q2, q2 ? 2 : 6);
#pragma unroll
    for (int i = 0; i < 4; ++i) { D.sa[i] = q01 ? sq[i] : sg[i]; D.sb[i] = q01 ? sq[i] : su[i]; D.sc[i] = q2 ? sq[i] : (wm ? su[i] : sg[i]); }
    D.stab = q01 ? 64 * 6144 : 64 * 7168; D.stc = q2 ? 64 * 6144 : 64 * 7168;
    D.co[0] = 0; D.co[1] = q01 ? 16 * 6144 : 32 * 7168; D.co[2] = q01 ? 32 * 6144 : 0; D.co[3] = q01 ? 48 * 6144 : 32 * 7168;
#pragma unroll
    for (int h = 0; h < 2; ++h)
#pragma unroll
      for (int i = 0; i < 4; ++i) D.lo[4 * h + i] = q01 ? h * 1024 + i * 3072 : K1_WG + (2 * wm + h) * 1024 + i * 7168;
    D.cc[0] = q2 ? (wm - 2) * 32 * 6144 : 0; D.cc[1] = D.cc[0] + (q2 ? 16 * 6144 : 32 * 7168);
    D.lo[8] = q2 ? 2048 + (wm - 2) * 6144 : K1_WG + wm * 14336 + 6144; D.lo[9] = D.lo[8] + (q2 ? 3072 : 7168);
#pragma unroll
    for (int s = 0; s < 10; ++s) {
      if (s < 8) dma16((s & 2) ? D.sb : D.sa, lb + K1_W0 + D.lo[s], s < 4 ? v0 : v1, D.co[s & 3]);
      else dma16(D.sc, lb + K1_W0 + D.lo[s], v2, D.cc[s - 8]);
    }
    if (wm < 2) dma16(mk_srd(wphi, 32 * 32 * 32 * 2), lb + K1_PHI + wm * 1024, lane * 16, c * 2048 + wm * 1024);
    k1_body<0>(wsum, t1, acc, lb, wm, lane, sx, v0, v1, v2, vxa, vxb, D);
    ST(t2 = mt_();)
    EPI_LANE();
    const int q = (lane & 15) >> 2, p = lane & 3;
    const unsigned wpb = lb + K1_PHI + (4 * g + q) * 64 + p * 8;
    const bf16x8 wp0 = trrd2(wpb, wpb + 16 * 64), wp1 = trrd2(wpb + 32, wpb + 16 * 64 + 32);
    if (rb == 0 && wm == 0) { ST16(wp0, WS_WPF + c * 2048); ST16(wp1, WS_WPF + c * 2048 + 1024); }
    const unsigned pkb = lb + wm * 8704;
    const unsigned pw = pkb + (lane & 15) * 136 + g * 8;
#pragma unroll
    for (int mt = 0; mt < 4; ++mt) {
      f32x4 pp0, pp1, pn0, pn1;
      featmap(pack8(acc[mt][0], acc[mt][1]), wp0, wp1, pp0, pp1, pn0, pn1);
      *(LAS u32x2*)(pw + mt * 16 * 136 + 0)  = (u32x2){pk2(pp0[0], pp0[1]), pk2(pp0[2], pp0[3])};
      *(LAS u32x2*)(pw + mt * 16 * 136 + 32) = (u32x2){pk2(pp1[0], pp1[1]), pk2(pp1[2], pp1[3])};
      *(LAS u32x2*)(pw + mt * 16 * 136 + 64) = (u32x2){pk2(pn0[0], pn0[1]), pk2(pn0[2], pn0[3])};
      *(LAS u32x2*)(pw + mt * 16 * 136 + 96) = (u32x2){pk2(pn1[0], pn1[1]), pk2(pn1[2], pn1[3])};
    }
    asm volatile("s_waitcnt lgkmcnt(0)" ::: "memory");
    f32x4 kv[4][2], zz[4];
#pragma unroll
    for (int ft = 0; ft < 4; ++ft) { kv[ft][0] = (f32x4){0.f, 0.f, 0.f, 0.f}; kv[ft][1] = kv[ft][0]; zz[ft] = kv[ft][0]; }
    const bf16x8 ones = {0x3f80, 0x3f80, 0x3f80, 0x3f80, 0x3f80, 0x3f80, 0x3f80, 0x3f80};
    const unsigned pr = pkb + (4 * g + q) * 136 + p * 8;
#pragma unroll
    for (int mp = 0; mp < 2; ++mp) {
      const bf16x8 va = pack8(acc[2 * mp][2], acc[2 * mp + 1][2]), vb = pack8(acc[2 * mp][3], acc[2 * mp + 1][3]);
#pragma unroll
      for (int ft = 0; ft < 4; ++ft) {
        const bf16x8 a = trrd2(pr + (32 * mp) * 136 + ft * 32, pr + (32 * mp + 16) * 136 + ft * 32);
        kv[ft][0] = mfma16(a, va, kv[ft][0]);
        kv[ft][1] = mfma16(a, vb, kv[ft][1]);
        zz[ft] = mfma16(a, ones, zz[ft]);
      }
    }
    const unsigned rd = lb + 65536 + wm * 8192 + lane * 16;
#pragma unroll
    for (int ft = 0; ft < 4; ++ft) {
      *(LAS f32x4*)(rd + (ft * 2 + 0) * 1024) = kv[ft][0];
      *(LAS f32x4*)(rd + (ft * 2 + 1) * 1024) = kv[ft][1];
    }
    { const int ft = lane & 3;
      const f32x4 zsel = ft == 0 ? zz[0] : (ft == 1 ? zz[1] : (ft == 2 ? zz[2] : zz[3]));
      *(LAS f32x4*)(lb + 65536 + 32768 + wm * 256 + ft * 64 + g * 16) = zsel; }
    const int jp = 7 * c + ((c & 1) ? 1 : 4), js = 7 * c + ((c & 1) ? 0 : 6);
    const unsigned hp = WS_AP + ((jp >> 1) * 128 + m16) * 1024, hs = WS_AP + ((js >> 1) * 128 + m16) * 1024 + (js & 1) * 8;
#pragma unroll
    for (int mt = 0; mt < 4; ++mt) {
      const f32x4 h0 = geglu4(acc[mt][4], acc[mt][7]), h1 = geglu4(acc[mt][5], acc[mt][8]), h2 = geglu4(acc[mt][6], acc[mt][9]);
      ST16(pack8(h0, h1), hp + mt * 1024);
      __builtin_amdgcn_raw_buffer_store_b64((u32x2){pk2(h2[0], h2[1]), pk2(h2[2], h2[3])}, rw, lane * 16 + hs + mt * 1024, 0, ST_AUX);
    }
  } else {
    {
      const unsigned vxa = (rb * 256 + 64 * wm + (lane >> 3)) * 2048 + (((lane & 7) ^ (lane >> 4)) << 4), vxb = vxa ^ 64;
#pragma unroll
      for (int j = 0; j < 8; ++j) dma16(sx, lb + K1_X0 + wm * 8192 + j * 1024, (j & 1) ? vxb : vxa, j * 16384);
    }
    const u32x4 sdn = mk_srd(wdown, DF * DM * 2), swo = mk_srd(wo, DM * DM * 2);
    const int r8 = 8 * rb + ((lane >> 2) & 7), l3 = lane & 3;
    const unsigned vx_ = (rb * 256 + (c & 3) * 64 + 32 * wm + (lane >> 1)) * 2048 + (lane & 1) * 64;
    const unsigned vg_ = r8 * 7168 + ((224 * c) & ~63) + l3 * 64;
    const unsigned vq_ = r8 * 6144 + (l3 == 3 ? 0 : l3) * 2048 + 64 * c;
    const unsigned v0 = wm < 2 ? vx_ : (wm == 2 ? vg_ : vq_);
    const unsigned v1 = wm == 2 ? vg_ : (wm < 2 ? (b * 7 + 4 * wm) * 4096 : b * 2 * 4096) + lane * 64;
#pragma unroll
    for (int i = 0; i < 4; ++i) { D.sa[i] = wm < 2 ? sx[i] : (wm == 2 ? sg[i] : sq[i]); D.sb[i] = wm < 2 ? sdn[i] : (wm == 2 ? su[i] : swo[i]); }
    D.stab = wm < 2 ? 128 : (wm == 2 ? 64 * 7168 : 64 * 6144); D.stc = wm == 2 ? 64 * 7168 : 4096;
    D.co[0] = 0; D.co[1] = 15; D.co[2] = wm == 2 ? 0 : (wm == 3 ? 6 : 4 + wm); D.co[3] = wm == 2 ? 15 : (wm == 3 ? 1 : 3 - wm);
    D.cc[0] = wm == 2 ? 128 : 43; D.cc[1] = wm == 2 ? 1 : 3;
    {
      u32x4 sb2 = D.sb; sb2[2] = wm == 2 ? sb2[2] : 0u;
#pragma unroll
      for (int t = 1; t < 3; ++t) { pfetch(D.sa, lb + K1_PF + wm * 512, v0, t * D.stab); pfetch(sb2, lb + K1_PF + wm * 512 + 256, v1, wm == 2 ? t * D.stc : 0u); }
    }
    k1_body<1>(wsum, t1, acc, lb, wm, lane, sx, v0, v1, 0u, 0u, 0u, D);
    ST(t2 = mt_();)
    EPI_LANE();
    const int grow = b * 8 + 2 * wm + (lane >> 5);
    u32x4 gx[4], gw[4];
#pragma unroll
    for (int i = 0; i < 4; ++i) {
      gx[i] = *(const u32x4*)(x + grow * DM + i * 256 + (lane & 31) * 8);
      gw[i] = *(const u32x4*)(gatew + i * 256 + (lane & 31) * 8);
    }
#pragma unroll
    for (int mt = 0; mt < 4; ++mt) ST16(pack8(acc[mt][0], acc[mt][1]), WS_QF + (c * 128 + m16 + mt) * 1024);
    const int j0 = 7 * c + ((c & 1) ? 3 : 0);
    const unsigned hp = WS_AP + ((j0 >> 1) * 128 + m16) * 1024;
#pragma unroll
    for (int mt = 0; mt < 4; ++mt) {
      const f32x4 h0 = geglu4(acc[mt][2], acc[mt][6]), h1 = geglu4(acc[mt][3], acc[mt][7]);
      ST16(pack8(h0, h1), hp + mt * 1024);
      const f32x4 h2 = geglu4(acc[mt][4], acc[mt][8]), h3 = geglu4(acc[mt][5], acc[mt][9]);
      ST16(pack8(h2, h3), hp + 131072 + mt * 1024);
    }
    {
      float s = 0.f;
#pragma unroll
      for (int i = 0; i < 4; ++i)
#pragma unroll
        for (int e = 0; e < 4; ++e) {
          const unsigned pr = pk2(__uint_as_float(gx[i][e] << 16) * __uint_as_float(gw[i][e] << 16), __uint_as_float(gx[i][e] & 0xffff0000u) * __uint_as_float(gw[i][e] & 0xffff0000u));
          s += __uint_as_float(pr << 16) + __uint_as_float(pr & 0xffff0000u);
        }
#pragma unroll
      for (int o = 1; o < 32; o <<= 1) s += __shfl_xor(s, o);
      s = bfr(s);
      const float gv = bfr(1.0f / (1.0f + __expf(-s)));
      if ((lane & 31) == 0) ((float*)(ws + WS_G))[grow] = gv;
      if (wm == 0 && lane == 0) { ((unsigned*)(ws + WS_FLAGS))[(8 + b) * 16] = 0u; if (b < 8) ((unsigned*)(ws + WS_FLAGS))[b * 16] = 0u; }
    }
  }
  LGKM_BAR();
  {
    int tid = w * 64 + __builtin_amdgcn_mbcnt_hi(~0u, __builtin_amdgcn_mbcnt_lo(~0u, 0u)); asm volatile("" : "+v"(tid));
    const unsigned rd = lb + 65536 + tid * 16;
    const f32x4 s = *(LAS f32x4*)(rd) + *(LAS f32x4*)(rd + 8192) + *(LAS f32x4*)(rd + 16384) + *(LAS f32x4*)(rd + 24576);
    __builtin_amdgcn_raw_buffer_store_b64((u32x2){pk2(s[0], s[1]), pk2(s[2], s[3])}, rw, WS_KVP + ((c * 8 + w) * 4 + (rb >> 1)) * 1024 + (tid & 63) * 16 + (rb & 1) * 8, 0, ST_AUX);
    if (tid < 16) {
      const unsigned rz = lb + 65536 + 32768 + tid * 16;
      const f32x4 zs = *(LAS f32x4*)(rz) + *(LAS f32x4*)(rz + 256) + *(LAS f32x4*)(rz + 512) + *(LAS f32x4*)(rz + 768);
      *(f32x4*)(ws + WS_ZP + ((size_t)(c * 8 + rb) * 16 + tid) * 16) = zs;
    }
  }
}

constexpr unsigned K2_SLOT = 16384, K2_KV = 131072, K2_LDS = 131072 + 4608;
constexpr int NS = 18, NH = 14;
#define OUT_AUX 16
#define VM_BAR(N) do { asm volatile("" ::: "memory"); __builtin_amdgcn_s_waitcnt(((N) & 15) | 0xF70 | (((N) >> 4) << 14)); __builtin_amdgcn_s_barrier(); asm volatile("" ::: "memory"); } while (0)
#define BAR() do { asm volatile("" ::: "memory"); __builtin_amdgcn_s_barrier(); asm volatile("" ::: "memory"); } while (0)

DI void wait_ge(const unsigned* flag, unsigned need) {
  for (unsigned spin = 0; spin < (1u << 22); ++spin) {
    if (__hip_atomic_load((const GAS unsigned*)flag, __ATOMIC_RELAXED, __HIP_MEMORY_SCOPE_AGENT) >= need) break;
    __builtin_amdgcn_s_sleep(1);
  }
}
#define lda(d, srd, voff, soff) d = __builtin_bit_cast(bf16x8, __builtin_amdgcn_raw_buffer_load_b128(srd, voff, soff, 16))

__global__ __launch_bounds__(768, 3) void k2_kernel(const uint16_t* __restrict__ wdown, const uint16_t* __restrict__ wo, char* __restrict__ ws,
                                                    uint16_t* __restrict__ out) {
  extern __shared__ __attribute__((aligned(16))) char smem[];
  const unsigned lb = (unsigned)(size_t)(LAS char*)smem;
  const int lane = threadIdx.x & 63;
  const int w = __builtin_amdgcn_readfirstlane(threadIdx.x >> 6);
  const int g = lane >> 4;
  const int b = blockIdx.x, xj = b & 7, bi = b >> 3;
  const int kq = xj >> 1;
  const int mt = 4 * (xj & 1) + (bi >> 3), nt128 = bi & 7;
  const int tile = mt * 8 + nt128;
  const int oc = 4 * xj + (bi & 3), orb = bi >> 2;
  unsigned* const flags = (unsigned*)(ws + WS_FLAGS);
  const float* const gs = (const float*)(ws + WS_G);
  auto tix = [&](int s) -> int { return s < NH ? NH * kq + s : 56 - NH + 4 * kq + s; };

  if (w >= 8) {
    const int sw = w - 8;
    const u32x4 sd = mk_srd(wdown, DF * DM * 2), so_ = mk_srd(wo, DM * DM * 2);
    const unsigned vb0 = (16 * sw + g) * 2048 + nt128 * 256 + (((lane & 15) ^ (g << 1)) << 4), vb1 = (vb0 ^ 128) + 4 * 2048;
    auto stage = [&](int s) {
      if (s < NS) {
        u32x4 r;
#pragma unroll
        for (int j = 0; j < 4; ++j) r[j] = s >= NH ? so_[j] : sd[j];
        const unsigned so = (s >= NH ? 4 * kq + s - NH : NH * kq + s) * 131072, dst = lb + (s & 7) * K2_SLOT + sw * 4096;
#pragma unroll
        for (int i = 0; i < 4; ++i) if (ABL != 12) dma16(r, dst + i * 1024, (i & 1) ? vb1 : vb0, so + (i >> 1) * 16384);
      }
    };
    stage(0); stage(1); stage(2); stage(3);
    VM_BAR(8);
    for (int k = 0; k < NS / 2 - 1; ++k) {
      stage(2 * k + 4); stage(2 * k + 5);
      if (k == 2 && threadIdx.x == 512)
        __hip_atomic_fetch_add((GAS unsigned*)(flags + orb * 16), 1u, __ATOMIC_RELAXED, __HIP_MEMORY_SCOPE_AGENT);
      if (k == 5 && sw == 3) {
        if (lane == 0) wait_ge(flags + mt * 16, 32u);
        asm volatile("s_waitcnt vmcnt(0)" ::: "memory");
      }
      if (2 * k + 4 < NS) VM_BAR(8); else VM_BAR(0);
    }
    BAR();
    if (sw == 0) {
      if (lane == 0) __hip_atomic_store((GAS unsigned*)(flags + (8 + tile * 4 + kq) * 16), 1u, __ATOMIC_RELAXED, __HIP_MEMORY_SCOPE_AGENT);
      if (lane < 4 && lane != kq) wait_ge(flags + (8 + tile * 4 + lane) * 16, 1u);
      asm volatile("s_waitcnt vmcnt(0)" ::: "memory");
      BAR();
    }
    return;
  }

  auto compute = [&](auto grp_) {
  constexpr int GRP = decltype(grp_)::value;
  ST(unsigned long long wsum = 0, r0 = rt_(), t0 = mt_();)
  const int q = (lane & 15) >> 2, p = lane & 3;
  const auto sa = __builtin_amdgcn_make_buffer_rsrc((void*)(ws + WS_AP), 0, 144u * 131072u, 0x00020000);
  const unsigned va = lane * 16;
  const unsigned abase = (mt * 16 + 2 * w) * 1024;
  const bf16x8 wp0 = *(const bf16x8*)(ws + WS_WPF + oc * 2048 + va), wp1 = *(const bf16x8*)(ws + WS_WPF + oc * 2048 + 1024 + va);
  bf16x8 qf[2]; float go[2]; u32x4 kvl[4];
#pragma unroll
  for (int i = 0; i < 2; ++i) {
    qf[i] = *(const bf16x8*)(ws + WS_QF + ((size_t)oc * 128 + orb * 16 + 2 * w + i) * 1024 + va);
    go[i] = gs[(orb * 16 + 2 * w + i) * 16 + (lane & 15)];
  }
#pragma unroll
  for (int r = 0; r < 4; ++r) kvl[r] = *(const u32x4*)(ws + WS_KVP + (size_t)(oc * 8 + w) * 4096 + r * 1024 + va);
  const char* zp = ws + WS_ZP + ((size_t)oc * 8 * 16 + 2 * g * 16 + (lane & 15)) * 16;
  const f32x4 za_ = *(const f32x4*)zp, zb_ = *(const f32x4*)(zp + 256);
  asm volatile("" ::: "memory");
  bf16x8 ar[4][2];
  { const unsigned a0 = abase + tix(0) * 262144, a1 = abase + tix(1) * 262144;
    lda(ar[0][0], sa, va, a0); lda(ar[0][1], sa, va, a0 + 1024);
    lda(ar[1][0], sa, va, a0 + 131072); lda(ar[1][1], sa, va, a0 + 131072 + 1024);
    lda(ar[2][0], sa, va, a1); lda(ar[2][1], sa, va, a1 + 1024); }
  const float gsc0 = gs[mt * 256 + w * 32 + (lane & 15)], gsc1 = gs[mt * 256 + w * 32 + 16 + (lane & 15)];
  asm volatile("" ::: "memory");
  bf16x8 pf[2][2];
#pragma unroll
  for (int i = 0; i < 2; ++i) { f32x4 pp0, pp1, pn0, pn1; featmap(qf[i], wp0, wp1, pp0, pp1, pn0, pn1); pf[i][0] = pack8(pp0, pp1); pf[i][1] = pack8(pn0, pn1); }
  {
    f32x4 kvs = {0.f, 0.f, 0.f, 0.f};
#pragma unroll
    for (int r = 0; r < 4; ++r) kvs += unpk_lo(kvl[r]) + unpk_hi(kvl[r]);
    *(LAS u32x2*)(lb + K2_KV + ((w & 1) * 2 + (w >> 2)) * 1024 + va + ((w >> 1) & 1) * 8) = (u32x2){pk2(kvs[0], kvs[1]), pk2(kvs[2], kvs[3])};
    const f32x4 zs = za_ + zb_;
    float z0 = zs[0], z1 = zs[1], z2 = zs[2], z3 = zs[3]; XRED2(z0, z1, fadd_); XRED2(z2, z3, fadd_);
    if (w == 0) *(LAS u32x2*)(lb + K2_KV + 4096 + ((lane >> 3) & 1) * 64 + (lane & 3) * 16 + ((lane >> 2) & 1) * 8) = (u32x2){pk2(z0, z1), pk2(z2, z3)};
  }
  f32x4 acc[2][8];
#pragma unroll
  for (int m = 0; m < 2; ++m)
#pragma unroll
    for (int n = 0; n < 8; ++n) acc[m][n] = (f32x4){0.f, 0.f, 0.f, 0.f};
  const unsigned bb0 = lb + (4 * g + q) * 256 + (((4 * (g & 1) + q) << 1 | (p >> 1)) << 4) + (p & 1) * 8;
  unsigned ba[8];
#pragma unroll
  for (int n = 0; n < 8; ++n) ba[n] = bb0 ^ (n << 5);
  unsigned sp2 = 0;
  bf16x8 bw[8];
  auto ldB = [&](int par, int u) -> bf16x8 { const unsigned a = ba[u & 7] + par * K2_SLOT + (u >> 3) * 8192; return trrd2(a, a + 4096); };
  __builtin_amdgcn_s_waitcnt(0xC07F);
  BAR();
  {
    bf16x8 ka[2][2], za[2];
#pragma unroll
    for (int ks = 0; ks < 2; ++ks) {
      ka[0][ks] = ldsrd(lb + K2_KV + (0 + ks) * 1024 + va);
      ka[1][ks] = ldsrd(lb + K2_KV + (2 + ks) * 1024 + va);
      za[ks] = ldsrd(lb + K2_KV + 4096 + ks * 64 + g * 16);
    }
#pragma unroll
    for (int i = 0; i < 2; ++i) {
      const f32x4 z4 = {0.f, 0.f, 0.f, 0.f};
      f32x4 n0 = mfma16(ka[0][1], pf[i][1], mfma16(ka[0][0], pf[i][0], z4));
      f32x4 n1 = mfma16(ka[1][1], pf[i][1], mfma16(ka[1][0], pf[i][0], z4));
      f32x4 dn = mfma16(za[1], pf[i][1], mfma16(za[0], pf[i][0], z4));
      const float sc = go[i] * __builtin_amdgcn_rcpf(dn[0] + 1e-6f);
      n0 *= sc; n1 *= sc;
      __builtin_amdgcn_raw_buffer_store_b128(__builtin_bit_cast(u32x4, pack8(n0, n1)), sa, va + ((112 + oc) * 128 + orb * 16 + 2 * w + i) * 1024, 0, 16  );
    }
    __builtin_amdgcn_s_waitcnt(0xC07F);
    asm volatile("" ::: "memory");
  }
  SB(); MFMA_FENCE();
  ST(unsigned long long t1 = mt_();)
#pragma unroll
  for (int u = 0; u < 6; ++u) bw[u] = ldB(0, u);
  auto step = [&](int s, auto par_, auto ia0_, auto ia1_, auto nxt_) {
    constexpr int par = decltype(par_)::value;
    constexpr bool ia0 = decltype(ia0_)::value, ia1 = decltype(ia1_)::value, nxt = decltype(nxt_)::value;
    const unsigned an1 = abase + tix(s + 1) * 262144 + 131072, an2 = abase + tix(s + 2) * 262144;
#pragma unroll
    for (int u = 0; u < 16; ++u) {
      if (u == 10 && nxt && par == 1) {
        VM_BAR(8);
        const unsigned nx = (sp2 + 32768) & 98304, dx = sp2 ^ nx; sp2 = nx;
#pragma unroll
        for (int n = 0; n < 8; ++n) { ba[n] ^= dx; asm volatile("" : "+v"(ba[n])); }
      }
      if (u < 10) bw[(u + 6) & 7] = ldB(par, u + 6);
      else if (nxt) bw[(u + 6) & 7] = ldB(par ^ 1, u - 10);
      if (ia0 && u < 2) lda(ar[(2 * par + 3) & 3][u], sa, va, an1 + u * 1024);
      if (ia1 && (u == 8 || u == 9)) lda(ar[2 * par][u - 8], sa, va, an2 + (u - 8) * 1024);
      if ((u & 1) == 0 && (u < 10 || nxt)) __builtin_amdgcn_s_waitcnt(0xCA7F);
#pragma unroll
      for (int m = 0; m < 2; ++m) mfma_ip(acc[m][u & 7], bw[u & 7], ar[2 * par + (u >> 3)][m]);
      SB();
    }
  };
#pragma unroll 1
  for (int s = 0; s < NH; s += 2) { step(s, I0{}, I1{}, I1{}, I1{}); step(s + 1, I1{}, I1{}, I1{}, I1{}); }
  {
    MFMA_FENCE(); SB();
    const float s0 = bfr(1.0f - gsc0), s1 = bfr(1.0f - gsc1);
#pragma unroll
    for (int n = 0; n < 8; ++n) { acc[0][n] *= s0; acc[1][n] *= s1; }
    SB(); MFMA_FENCE();
  }
  step(NH, I0{}, I1{}, I1{}, I1{}); step(NH + 1, I1{}, I1{}, I1{}, I1{});
  ST(wsum = mt_();)
  step(NH + 2, I0{}, I1{}, I0{}, I1{});
  step(NH + 3, I1{}, I0{}, I0{}, I0{});
  MFMA_FENCE(); SB();
  ST(unsigned long long t2 = mt_();)
  const auto rp = __builtin_amdgcn_make_buffer_rsrc((void*)(ws + WS_PART), 0, 64u * 262144u, 0x00020000);
  const unsigned ex = lb + 32768;
  if ((w >> 1) == kq) {
#pragma unroll
    for (int m = 0; m < 2; ++m)
#pragma unroll
      for (int n = 0; n < 8; ++n) *(LAS f32x4*)(ex + (((w & 1) * 2 + m) * 8 + n) * 1024 + va) = acc[m][n];
  } else {
#pragma unroll
    for (int m = 0; m < 2; ++m)
#pragma unroll
      for (int n = 0; n < 4; ++n)
        __builtin_amdgcn_raw_buffer_store_b128(__builtin_bit_cast(u32x4, pack8(acc[m][2 * n], acc[m][2 * n + 1])), rp, va + (tile * 4 + kq) * 65536 + w * 8192 + m * 4096 + n * 1024, 0, 16);
  }
  ST(unsigned long long e0 = mt_();)
  WAIT_BAR(0);
  ST(unsigned long long e1 = mt_();)
  f32x4 fin[4];
#pragma unroll
  for (int n = 0; n < 4; ++n) fin[n] = *(LAS f32x4*)(ex + ((w >> 1) * 8 + 4 * (w & 1) + n) * 1024 + va);
  BAR();
  ST(unsigned long long e2 = mt_();)
  {
    u32x4 part[3][2];
#pragma unroll
    for (int j = 0; j < 3; ++j) {
      const int src = (kq + 1 + j) & 3;
      const unsigned po = (tile * 4 + src) * 65536 + (2 * kq + (w >> 2)) * 8192 + ((w >> 1) & 1) * 4096 + 2 * (w & 1) * 1024;
      part[j][0] = __builtin_amdgcn_raw_buffer_load_b128(rp, va, po, 16);
      part[j][1] = __builtin_amdgcn_raw_buffer_load_b128(rp, va, po + 1024, 16);
    }
    const auto ro = __builtin_amdgcn_make_buffer_rsrc((void*)out, 0, SEQ * DM * 2, 0x00020000);
    const unsigned oo = ((mt * 256 + kq * 64 + (w >> 1) * 16 + (lane & 15)) * DM + nt128 * 128 + (w & 1) * 64 + (g & 1) * 16 + (g >> 1) * 8) * 2;
#pragma unroll
    for (int n = 0; n < 2; ++n) {
      const f32x4 v0 = fin[2 * n] + (unpk_lo(part[0][n]) + unpk_lo(part[1][n]) + unpk_lo(part[2][n]));
      const f32x4 v1 = fin[2 * n + 1] + (unpk_hi(part[0][n]) + unpk_hi(part[1][n]) + unpk_hi(part[2][n]));
      const auto r0 = __builtin_amdgcn_permlane16_swap(pk2(v0[0], v0[1]), pk2(v1[0], v1[1]), false, false);
      const auto r1 = __builtin_amdgcn_permlane16_swap(pk2(v0[2], v0[3]), pk2(v1[2], v1[3]), false, false);
      __builtin_amdgcn_raw_buffer_store_b128((u32x4){r0[0], r1[0], r0[1], r1[1]}, ro, oo + n * 64, 0, OUT_AUX);
    }
  }
  };
  compute(I0{});
}

extern "C" void kernel_launch(void* const* d_in, const int* in_sizes, int n_in, void* d_out, int out_size, void* d_ws, size_t ws_size,
                              hipStream_t stream) {
  const uint16_t* x = (const uint16_t*)d_in[0];
  const uint16_t* wqkv = (const uint16_t*)d_in[1];
  const uint16_t* wphi = (const uint16_t*)d_in[2];
  const uint16_t* wo = (const uint16_t*)d_in[3];
  const uint16_t* wg = (const uint16_t*)d_in[5];
  const uint16_t* wu = (const uint16_t*)d_in[6];
  const uint16_t* wd = (const uint16_t*)d_in[7];
  const uint16_t* gw = (const uint16_t*)d_in[8];
  (void)hipFuncSetAttribute((const void*)k1_kernel, hipFuncAttributeMaxDynamicSharedMemorySize, K1_LDS);
  (void)hipFuncSetAttribute((const void*)k2_kernel, hipFuncAttributeMaxDynamicSharedMemorySize, K2_LDS);
  k1_kernel<<<256, 512, K1_LDS, stream>>>(x, wqkv, wphi, wg, wu, gw, wd, wo, (char*)d_ws);
  k2_kernel<<<256, 768, K2_LDS, stream>>>(wd, wo, (char*)d_ws, (uint16_t*)d_out);
}
```

```cpp
#include <hip/hip_runtime.h>
#include <hip/hip_bf16.h>
#include <stdint.h>
#include <type_traits>
#pragma clang diagnostic ignored "-Wint-to-pointer-cast"

typedef short bf16x8 __attribute__((ext_vector_type(8)));
typedef short s16x4 __attribute__((ext_vector_type(4)));
typedef float f32x4 __attribute__((ext_vector_type(4)));
typedef float f32x2 __attribute__((ext_vector_type(2)));
typedef unsigned u32x4 __attribute__((ext_vector_type(4)));
typedef unsigned u32x2 __attribute__((ext_vector_type(2)));
typedef __bf16 bf16x2_t __attribute__((ext_vector_type(2)));
#define LAS __attribute__((address_space(3)))
#define GAS __attribute__((address_space(1)))
#define DI __device__ __forceinline__

constexpr int SEQ = 2048, DM = 1024, DF = 3584;
constexpr size_t WS_FLAGS = 0;
constexpr size_t WS_G     = 32768;
constexpr size_t WS_ZP    = 65536;
constexpr size_t WS_KVP   = 262144;
constexpr size_t WS_WPF   = 3u << 20;
constexpr size_t WS_QF    = 4u << 20;
constexpr size_t WS_AP    = 16u << 20;
constexpr size_t WS_PART  = 40u << 20;

#define ABL 0
DI f32x4 mfma16(bf16x8 a, bf16x8 b, f32x4 c) { return __builtin_amdgcn_mfma_f32_16x16x32_bf16(a, b, c, 0, 0, 0); }
DI void mfma_ip(f32x4& c, bf16x8 a, bf16x8 b) { asm volatile("v_mfma_f32_16x16x32_bf16 %0, %1, %2, %0" : "+v"(c) : "v"(a), "v"(b)); }
#define MFMA_FENCE() asm volatile("s_nop 7\n\ts_nop 7" ::: "memory")
DI s16x4 trrd(unsigned a) { return __builtin_amdgcn_ds_read_tr16_b64_v4i16((LAS s16x4*)a); }
DI bf16x8 trrd2(unsigned a0, unsigned a1) { s16x4 lo = trrd(a0), hi = trrd(a1); return __builtin_shufflevector(lo, hi, 0, 1, 2, 3, 4, 5, 6, 7); }
DI bf16x8 ldsrd(unsigned a) { return *(LAS bf16x8*)a; }
DI unsigned pk2(float lo, float hi) { f32x2 v = {lo, hi}; bf16x2_t b = __builtin_convertvector(v, bf16x2_t); return __builtin_bit_cast(unsigned, b); }
DI bf16x8 pack8(f32x4 a, f32x4 b) { u32x4 r = {pk2(a[0], a[1]), pk2(a[2], a[3]), pk2(b[0], b[1]), pk2(b[2], b[3])}; return __builtin_bit_cast(bf16x8, r); }
DI f32x4 unpk_lo(u32x4 v) { return (f32x4){__uint_as_float(v[0] << 16), __uint_as_float(v[0] & 0xffff0000u), __uint_as_float(v[1] << 16), __uint_as_float(v[1] & 0xffff0000u)}; }
DI f32x4 unpk_hi(u32x4 v) { return (f32x4){__uint_as_float(v[2] << 16), __uint_as_float(v[2] & 0xffff0000u), __uint_as_float(v[3] << 16), __uint_as_float(v[3] & 0xffff0000u)}; }
DI float bfr(float x) { unsigned u = pk2(x, x); return __uint_as_float(u << 16); }
DI u32x4 mk_srd(const void* p, unsigned bytes) {
  uint64_t a = (uint64_t)p;
  u32x4 r;
  r[0] = __builtin_amdgcn_readfirstlane((unsigned)a);
  r[1] = __builtin_amdgcn_readfirstlane((unsigned)(a >> 32) & 0xffffu);
  r[2] = __builtin_amdgcn_readfirstlane(bytes);
  r[3] = 0x00020000u;
  return r;
}
DI void pfetch(u32x4 srd, unsigned lds, unsigned voff, unsigned soff) {
  asm volatile("s_mov_b32 m0, %0\n\ts_nop 0\n\tbuffer_load_dword %1, %2, %3 offen lds" ::"s"(lds), "v"(voff), "s"(srd), "s"(soff) : "memory");
}
DI void dma16(u32x4 srd, unsigned lds, unsigned voff, unsigned soff) {
  asm volatile("s_mov_b32 m0, %0\n\ts_nop 0\n\tbuffer_load_dwordx4 %1, %2, %3 offen lds" ::"s"(lds), "v"(voff), "s"(srd), "s"(soff) : "memory");
}
#define WAIT_BAR(N) do { asm volatile("" ::: "memory"); __builtin_amdgcn_s_waitcnt(((N) & 15) | 0x70 | (((N) >> 4) << 14)); __builtin_amdgcn_s_barrier(); asm volatile("" ::: "memory"); } while (0)
#define LGKM_BAR() do { asm volatile("" ::: "memory"); __builtin_amdgcn_s_waitcnt(0xC07F); __builtin_amdgcn_s_barrier(); asm volatile("" ::: "memory"); } while (0)
#define SB() __builtin_amdgcn_sched_barrier(0)
#define ST(...)
#define WAIT_BAR_T(N) WAIT_BAR(N)

DI float fmx(float a, float b) { return __builtin_amdgcn_fmed3f(a, b, __builtin_inff()); }
#define XRED2(a, b, OP) do { \
    auto r1_ = __builtin_amdgcn_permlane16_swap(__float_as_uint(a), __float_as_uint(b), false, false); \
    float m_ = OP(__uint_as_float(r1_[0]), __uint_as_float(r1_[1])); \
    auto r2_ = __builtin_amdgcn_permlane32_swap(__float_as_uint(m_), __float_as_uint(m_), false, false); \
    m_ = OP(__uint_as_float(r2_[0]), __uint_as_float(r2_[1])); \
    auto r3_ = __builtin_amdgcn_permlane16_swap(__float_as_uint(m_), __float_as_uint(m_), false, false); \
    a = __uint_as_float(r3_[0]); b = __uint_as_float(r3_[1]); } while (0)
DI float fadd_(float a, float b) { return a + b; }
DI f32x4 exp2v(f32x4 v) { return (f32x4){__builtin_amdgcn_exp2f(v[0]), __builtin_amdgcn_exp2f(v[1]), __builtin_amdgcn_exp2f(v[2]), __builtin_amdgcn_exp2f(v[3])}; }

DI void featmap(bf16x8 fr, bf16x8 wp0, bf16x8 wp1, f32x4& pp0, f32x4& pp1, f32x4& pn0, f32x4& pn1) {
  const f32x4 z4 = {0.f, 0.f, 0.f, 0.f};
  constexpr float L2E = 1.4426950408889634f;
  const f32x4 t0 = mfma16(wp0, fr, z4) * L2E, t1 = mfma16(wp1, fr, z4) * L2E;
  float mx = fmaxf(fmaxf(fmaxf(t0[0], t0[1]), fmaxf(t0[2], t0[3])), fmaxf(fmaxf(t1[0], t1[1]), fmaxf(t1[2], t1[3])));
  float nm = -fminf(fminf(fminf(t0[0], t0[1]), fminf(t0[2], t0[3])), fminf(fminf(t1[0], t1[1]), fminf(t1[2], t1[3])));
  XRED2(mx, nm, fmx);
  pp0 = exp2v(t0 - mx); pp1 = exp2v(t1 - mx);
  pn0 = exp2v(-nm - t0); pn1 = exp2v(-nm - t1);
  const f32x4 s4p = pp0 + pp1, s4n = pn0 + pn1;
  float sp = (s4p[0] + s4p[1]) + (s4p[2] + s4p[3]), sn = (s4n[0] + s4n[1]) + (s4n[2] + s4n[3]);
  XRED2(sp, sn, fadd_);
  const float ip = __builtin_amdgcn_rcpf(sp), in = __builtin_amdgcn_rcpf(sn);
  pp0 *= ip; pp1 *= ip; pn0 *= in; pn1 *= in;
}

DI f32x4 geglu4(f32x4 g, f32x4 u) {
  constexpr float C1 = -2.0f * 0.7978845608028654f * 1.4426950408889634f, C2 = C1 * 0.044715f;
  const f32x4 d = exp2v(g * (g * g * C2 + C1)) + 1.0f;
  const f32x4 r = {__builtin_amdgcn_rcpf(d[0]), __builtin_amdgcn_rcpf(d[1]), __builtin_amdgcn_rcpf(d[2]), __builtin_amdgcn_rcpf(d[3])};
  return (g * u) * r;
}
#define ST_AUX 16
DI void prio_if(int hi) { asm volatile("s_setprio 0\n\ts_cmp_eq_u32 %0, 0\n\ts_cbranch_scc1 1f\n\ts_setprio 1\n1:" ::"s"(hi) : "scc"); }
#define PRIO2 0
#define PRIO1 0
using I0 = std::integral_constant<int, 0>; using I1 = std::integral_constant<int, 1>;

#define K1STG(x) x
constexpr unsigned K1_X0 = 0, K1_XS = K1STG(32768), K1_W0 = 65536, K1_WS = K1STG(40960), K1_WG = 12288, K1_WU = 26624, K1_PHI = 147456, K1_PF = 149504, K1_LDS = 151552 + 1024;

#define WR 5
#define PFU 4
struct K1Dma {
  u32x4 sa, sb, sc;
  unsigned co[4], cc[2];
  unsigned lo[10];
  unsigned stab, stc;
};

template <int WN>
DI void k1_body(unsigned long long& wsum, unsigned long long& tfirst, f32x4 (&acc)[4][10], const unsigned lb, const int wm, const int lane,
                const u32x4 sx, const unsigned v0, const unsigned v1, const unsigned v2, const unsigned vxa, const unsigned vxb, const K1Dma& D) {
  const int g = lane >> 4, q = (lane & 15) >> 2, p = lane & 3, fr = lane & 15;
  const unsigned xrow = wm * 64 + fr;
  const unsigned xb0 = lb + K1_X0 + xrow * 128 + ((g ^ ((xrow >> 1) & 7)) << 4);
  const unsigned xb1 = xb0 ^ 64;
  const unsigned qb0 = lb + K1_W0 + (8 * g + q) * 192 + ((0 ^ (g & 1)) << 5) + p * 8;
  const unsigned qb1 = lb + K1_W0 + (8 * g + q) * 192 + ((1 ^ (g & 1)) << 5) + p * 8;
  const unsigned gb0 = lb + K1_W0 + K1_WG + (8 * g + 4 * (0 ^ (g & 1)) + q) * 224 + p * 8;
  const unsigned gb1 = lb + K1_W0 + K1_WG + (8 * g + 4 * (1 ^ (g & 1)) + q) * 224 + p * 8;
  const unsigned xl = lb + K1_X0 + wm * 8192, wl = lb + K1_W0;
  unsigned sxo = 128, tab = D.stab, tc = D.stc;
  int pt = 3;

  bf16x8 xf[2][4], wf[WR];
  auto ldW = [&](int st, int u) -> bf16x8 {
    const int ks = u / 10, i = u % 10;
    const unsigned so = st * K1_WS;
    constexpr int NQ = WN == 0 ? 4 : 2;
    constexpr int NG = WN == 0 ? 3 : 4;
    constexpr int P0 = WN == 0 ? 4 : 0;
    if (i < NQ) {
      const int ptl = WN == 0 ? 1 + (i >> 1) : 0, nh = i & 1;
      const unsigned b = (nh ? qb1 : qb0) + so + ks * 32 * 192 + ptl * 64;
      return trrd2(b, b + 4 * 192);
    } else {
      const int j = i - NQ, up = j / NG, P = P0 + j % NG;
      const unsigned o = so + ks * 32 * 224 + up * (K1_WU - K1_WG) + P * 32;
      return trrd2(gb0 + o, gb1 + o);
    }
  };
  auto ldX = [&](int st, int ks, int mt) -> bf16x8 { return ldsrd((ks ? xb1 : xb0) + st * K1_XS + mt * 2048); };
  auto mma = [&](int st, int u) {
    const int ks = u / 10, i = u % 10, ri = (st * 20 + u) % WR;
#pragma unroll
    for (int mt = 0; mt < 4; ++mt) {
      if (WN == 0 && (i == 2 || i == 3)) mfma_ip(acc[mt][i], xf[ks][mt], wf[ri]);
      else mfma_ip(acc[mt][i], wf[ri], xf[ks][mt]);
    }
  };
  auto dmaX = [&](int st, int j) { if (ABL != 10) dma16(sx, xl + st * K1_XS + j * 1024, (j & 1) ? vxb : vxa, sxo + j * 16384); };
  auto dmaW = [&](int st, int s) {
    if (ABL == 10) return;
    if (s < 8) dma16((s & 2) ? D.sb : D.sa, wl + st * K1_WS + D.lo[s], s < 4 ? v0 : v1, tab + D.co[s & 3]);
    else dma16(D.sc, wl + st * K1_WS + D.lo[s], v2, tc + D.cc[s - 8]);
  };
  auto warm = [&](int k) {   if (ABL == 10) return;
    const unsigned i0 = (unsigned)(pt - (int)D.co[2 * k]), i = k ? (i0 * D.cc[0]) >> 7 : i0;
    const bool on = i <= D.co[2 * k + 1] && (!k || i * D.cc[1] == i0);
    u32x4 r = k ? D.sb : D.sa;
    r[2] = on ? r[2] : 0u;
    pfetch(r, lb + K1_PF + wm * 512 + k * 256, k ? v1 : v0, (on ? i : 0u) * (k ? D.stc : D.stab));
  };

  constexpr int WD = WR - 1, UB = 20 - WD;
  if (WN == 0) {
#pragma unroll
    for (int sl = 0; sl < 19 - UB; ++sl) {
      if (sl < 5) { dmaW(1, 2 * sl); dmaW(1, 2 * sl + 1); } else { dmaX(1, 2 * (sl - 5)); dmaX(1, 2 * (sl - 5) + 1); }
    }
    WAIT_BAR(ABL == 10 || ABL == 1 || ABL == 5 ? 0 : 2 * (19 - UB));
  } else WAIT_BAR(ABL == 4 || ABL == 5 ? 0 : 4);
  ST(tfirst = mt_();)
#pragma unroll
  for (int mt = 0; mt < 4; ++mt) xf[0][mt] = ldX(0, 0, mt);
#pragma unroll
  for (int u = 0; u < WD; ++u) wf[u] = ldW(0, u);
  SB(); MFMA_FENCE();

  auto step = [&](auto st_, auto dox_, auto dow_) {
    constexpr int st = decltype(st_)::value;
    constexpr bool dox = decltype(dox_)::value, dow = decltype(dow_)::value;
#pragma unroll
    for (int u = 0; u < 20; ++u) {
      if (u == UB + 1) {
        if (WN == 0) { sxo += 128; tab += D.stab; tc += D.stc; } else ++pt;
        if (WN == 0) WAIT_BAR_T(0); else WAIT_BAR_T(63);
        if (dox) wf[(st * 20 + UB + WD) % WR] = ldW(st ^ 1, 0);
      }
      if (u < UB) wf[(st * 20 + u + WD) % WR] = ldW(st, u + WD);
      else if (u > UB && dox) wf[(st * 20 + u + WD) % WR] = ldW(st ^ 1, u - UB);
      if (u >= 2 && u < 6) xf[1][u - 2] = ldX(st, 1, u - 2);
      if (u >= 16 && u > UB && dox) { if (UB >= 16) { if (u == UB + 1) { for (int mt = 0; mt < 4; ++mt) xf[0][mt] = ldX(st ^ 1, 0, mt); } } else xf[0][u - 16] = ldX(st ^ 1, 0, u - 16); }
      if (WN == 0) {
        const int sl = u > UB ? u - UB - 1 : u + 19 - UB;
        const bool post = u > UB;
        if (sl < 5) { if (post ? dow : dox) { dmaW(post ? st : st ^ 1, 2 * sl); dmaW(post ? st : st ^ 1, 2 * sl + 1); } }
        else if (sl < 9) { if (post ? dow : dox) { dmaX(post ? st : st ^ 1, 2 * (sl - 5)); dmaX(post ? st : st ^ 1, 2 * (sl - 5) + 1); } }
      }
      if (WN == 1 && dow && (u == PFU || u == PFU + 2)) warm(u != PFU);
      mma(st, u);
      SB();
    }
  };
  for (int t2 = 0; t2 < 7; ++t2) { step(I0{}, I1{}, I1{}); step(I1{}, I1{}, I1{}); }
  step(I0{}, I1{}, I0{});
  step(I1{}, I0{}, I0{});
  MFMA_FENCE(); SB();
}

__global__ __launch_bounds__(512, 2) void k1_kernel(const uint16_t* __restrict__ x, const uint16_t* __restrict__ wqkv, const uint16_t* __restrict__ wphi,
                                                    const uint16_t* __restrict__ wg, const uint16_t* __restrict__ wu, const uint16_t* __restrict__ gatew,
                                                    const uint16_t* __restrict__ wdown, const uint16_t* __restrict__ wo, char* __restrict__ ws) {
  extern __shared__ __attribute__((aligned(16))) char smem[];
  const unsigned lb = (unsigned)(size_t)(LAS char*)smem;
  unsigned long long wsum = 0; ST(unsigned long long r0 = rt_(); unsigned long long t0 = mt_(); unsigned long long t2 = 0;)
  const int tid = threadIdx.x, lane = tid & 63;
  const int w = __builtin_amdgcn_readfirstlane(tid >> 6);
  const int wn = w >> 2, wm = w & 3;
  const int b = blockIdx.x, xj = b & 7, bi = b >> 3;
  const int c = 4 * xj + (bi & 3), rb = bi >> 2;
  const u32x4 sx = mk_srd(x, SEQ * DM * 2);
  const u32x4 sq = mk_srd(wqkv, DM * 3072 * 2), sg = mk_srd(wg, DM * DF * 2), su = mk_srd(wu, DM * DF * 2);
  f32x4 acc[4][10];
#pragma unroll
  for (int m = 0; m < 4; ++m)
#pragma unroll
    for (int i = 0; i < 10; ++i) acc[m][i] = (f32x4){0.f, 0.f, 0.f, 0.f};
  K1Dma D;
  const int m16 = rb * 16 + wm * 4;
  const auto rw = __builtin_amdgcn_make_buffer_rsrc((void*)ws, 0, 64u << 20, 0x00020000);
#define EPI_LANE() int lane = __builtin_amdgcn_mbcnt_hi(~0u, __builtin_amdgcn_mbcnt_lo(~0u, 0u)); asm volatile("" : "+v"(lane)); const int g = lane >> 4;
#define ST16(val, so) __builtin_amdgcn_raw_buffer_store_b128(__builtin_bit_cast(u32x4, val), rw, lane * 16 + (unsigned)(so), 0, ST_AUX)
  unsigned long long t1 = 0;
  if (wn == 0) {
    const unsigned vxa = (rb * 256 + 64 * wm + (lane >> 3)) * 2048 + (((lane & 7) ^ (lane >> 4)) << 4), vxb = vxa ^ 64;
    const unsigned gtbl = (c & 1) ? 0x0216543u : 0x6543210u;
    auto pat = [&](bool isq, int pi) -> unsigned {
      const int ci = 64 * pi + lane, r = (ci * (isq ? 5462 : 4682)) >> 16, cc = ci - (isq ? 12 : 14) * r;
      const int o8 = (r >> 3) & 1;
      const unsigned vq = r * 6144 + ((cc >> 2) * 1024 + 32 * c + (((cc >> 1) & 1) ^ o8) * 16 + (cc & 1) * 8) * 2;
      const unsigned vg = (r ^ (o8 << 2)) * 7168 + (112 * c + 16 * ((gtbl >> (4 * (cc >> 1))) & 7) + 8 * (cc & 1)) * 2;
      return isq ? vq : vg;
    };
    const bool q01 = wm == 3, q2 = wm >= 2;
    const unsigned v0 = pat(q01, q01 ? 0 : 2 * wm), v1 = pat(q01, q01 ? 1 : 2 * wm + 1), v2 = pat(q2, q2 ? 2 : 6);
#pragma unroll
    for (int i = 0; i < 4; ++i) { D.sa[i] = q01 ? sq[i] : sg[i]; D.sb[i] = q01 ? sq[i] : su[i]; D.sc[i] = q2 ? sq[i] : (wm ? su[i] : sg[i]); }
    D.stab = q01 ? 64 * 6144 : 64 * 7168; D.stc = q2 ? 64 * 6144 : 64 * 7168;
    D.co[0] = 0; D.co[1] = q01 ? 16 * 6144 : 32 * 7168; D.co[2] = q01 ? 32 * 6144 : 0; D.co[3] = q01 ? 48 * 6144 : 32 * 7168;
#pragma unroll
    for (int h = 0; h < 2; ++h)
#pragma unroll
      for (int i = 0; i < 4; ++i) D.lo[4 * h + i] = q01 ? h * 1024 + i * 3072 : K1_WG + (2 * wm + h) * 1024 + i * 7168;
    D.cc[0] = q2 ? (wm - 2) * 32 * 6144 : 0; D.cc[1] = D.cc[0] + (q2 ? 16 * 6144 : 32 * 7168);
    D.lo[8] = q2 ? 2048 + (wm - 2) * 6144 : K1_WG + wm * 14336 + 6144; D.lo[9] = D.lo[8] + (q2 ? 3072 : 7168);
#pragma unroll
    for (int s = 0; s < 10; ++s) {
      if (s < 8) dma16((s & 2) ? D.sb : D.sa, lb + K1_W0 + D.lo[s], s < 4 ? v0 : v1, D.co[s & 3]);
      else dma16(D.sc, lb + K1_W0 + D.lo[s], v2, D.cc[s - 8]);
    }
    if (wm < 2) dma16(mk_srd(wphi, 32 * 32 * 32 * 2), lb + K1_PHI + wm * 1024, lane * 16, c * 2048 + wm * 1024);
    k1_body<0>(wsum, t1, acc, lb, wm, lane, sx, v0, v1, v2, vxa, vxb, D);
    ST(t2 = mt_();)
    EPI_LANE();
    const int q = (lane & 15) >> 2, p = lane & 3;
    const unsigned wpb = lb + K1_PHI + (4 * g + q) * 64 + p * 8;
    const bf16x8 wp0 = trrd2(wpb, wpb + 16 * 64), wp1 = trrd2(wpb + 32, wpb + 16 * 64 + 32);
    if (rb == 0 && wm == 0) { ST16(wp0, WS_WPF + c * 2048); ST16(wp1, WS_WPF + c * 2048 + 1024); }
    const unsigned pkb = lb + wm * 8704;
    const unsigned pw = pkb + (lane & 15) * 136 + g * 8;
#pragma unroll
    for (int mt = 0; mt < 4; ++mt) {
      f32x4 pp0, pp1, pn0, pn1;
      featmap(pack8(acc[mt][0], acc[mt][1]), wp0, wp1, pp0, pp1, pn0, pn1);
      *(LAS u32x2*)(pw + mt * 16 * 136 + 0)  = (u32x2){pk2(pp0[0], pp0[1]), pk2(pp0[2], pp0[3])};
      *(LAS u32x2*)(pw + mt * 16 * 136 + 32) = (u32x2){pk2(pp1[0], pp1[1]), pk2(pp1[2], pp1[3])};
      *(LAS u32x2*)(pw + mt * 16 * 136 + 64) = (u32x2){pk2(pn0[0], pn0[1]), pk2(pn0[2], pn0[3])};
      *(LAS u32x2*)(pw + mt * 16 * 136 + 96) = (u32x2){pk2(pn1[0], pn1[1]), pk2(pn1[2], pn1[3])};
    }
    asm volatile("s_waitcnt lgkmcnt(0)" ::: "memory");
    f32x4 kv[4][2], zz[4];
#pragma unroll
    for (int ft = 0; ft < 4; ++ft) { kv[ft][0] = (f32x4){0.f, 0.f, 0.f, 0.f}; kv[ft][1] = kv[ft][0]; zz[ft] = kv[ft][0]; }
    const bf16x8 ones = {0x3f80, 0x3f80, 0x3f80, 0x3f80, 0x3f80, 0x3f80, 0x3f80, 0x3f80};
    const unsigned pr = pkb + (4 * g + q) * 136 + p * 8;
#pragma unroll
    for (int mp = 0; mp < 2; ++mp) {
      const bf16x8 va = pack8(acc[2 * mp][2], acc[2 * mp + 1][2]), vb = pack8(acc[2 * mp][3], acc[2 * mp + 1][3]);
#pragma unroll
      for (int ft = 0; ft < 4; ++ft) {
        const bf16x8 a = trrd2(pr + (32 * mp) * 136 + ft * 32, pr + (32 * mp + 16) * 136 + ft * 32);
        kv[ft][0] = mfma16(a, va, kv[ft][0]);
        kv[ft][1] = mfma16(a, vb, kv[ft][1]);
        zz[ft] = mfma16(a, ones, zz[ft]);
      }
    }
    const unsigned rd = lb + 65536 + wm * 8192 + lane * 16;
#pragma unroll
    for (int ft = 0; ft < 4; ++ft) {
      *(LAS f32x4*)(rd + (ft * 2 + 0) * 1024) = kv[ft][0];
      *(LAS f32x4*)(rd + (ft * 2 + 1) * 1024) = kv[ft][1];
    }
    { const int ft = lane & 3;
      const f32x4 zsel = ft == 0 ? zz[0] : (ft == 1 ? zz[1] : (ft == 2 ? zz[2] : zz[3]));
      *(LAS f32x4*)(lb + 65536 + 32768 + wm * 256 + ft * 64 + g * 16) = zsel; }
    const int jp = 7 * c + ((c & 1) ? 1 : 4), js = 7 * c + ((c & 1) ? 0 : 6);
    const unsigned hp = WS_AP + ((jp >> 1) * 128 + m16) * 1024, hs = WS_AP + ((js >> 1) * 128 + m16) * 1024 + (js & 1) * 8;
#pragma unroll
    for (int mt = 0; mt < 4; ++mt) {
      const f32x4 h0 = geglu4(acc[mt][4], acc[mt][7]), h1 = geglu4(acc[mt][5], acc[mt][8]), h2 = geglu4(acc[mt][6], acc[mt][9]);
      ST16(pack8(h0, h1), hp + mt * 1024);
      __builtin_amdgcn_raw_buffer_store_b64((u32x2){pk2(h2[0], h2[1]), pk2(h2[2], h2[3])}, rw, lane * 16 + hs + mt * 1024, 0, ST_AUX);
    }
  } else {
    {
      const unsigned vxa = (rb * 256 + 64 * wm + (lane >> 3)) * 2048 + (((lane & 7) ^ (lane >> 4)) << 4), vxb = vxa ^ 64;
#pragma unroll
      for (int j = 0; j < 8; ++j) dma16(sx, lb + K1_X0 + wm * 8192 + j * 1024, (j & 1) ? vxb : vxa, j * 16384);
    }
    const u32x4 sdn = mk_srd(wdown, DF * DM * 2), swo = mk_srd(wo, DM * DM * 2);
    const int r8 = 8 * rb + ((lane >> 2) & 7), l3 = lane & 3;
    const unsigned vx_ = (rb * 256 + (c & 3) * 64 + 32 * wm + (lane >> 1)) * 2048 + (lane & 1) * 64;
    const unsigned vg_ = r8 * 7168 + ((224 * c) & ~63) + l3 * 64;
    const unsigned vq_ = r8 * 6144 + (l3 == 3 ? 0 : l3) * 2048 + 64 * c;
    const unsigned v0 = wm < 2 ? vx_ : (wm == 2 ? vg_ : vq_);
    const unsigned v1 = wm == 2 ? vg_ : (wm < 2 ? (b * 7 + 4 * wm) * 4096 : b * 2 * 4096) + lane * 64;
#pragma unroll
    for (int i = 0; i < 4; ++i) { D.sa[i] = wm < 2 ? sx[i] : (wm == 2 ? sg[i] : sq[i]); D.sb[i] = wm < 2 ? sdn[i] : (wm == 2 ? su[i] : swo[i]); }
    D.stab = wm < 2 ? 128 : (wm == 2 ? 64 * 7168 : 64 * 6144); D.stc = wm == 2 ? 64 * 7168 : 4096;
    D.co[0] = 0; D.co[1] = 15; D.co[2] = wm == 2 ? 0 : (wm == 3 ? 6 : 4 + wm); D.co[3] = wm == 2 ? 15 : (wm == 3 ? 1 : 3 - wm);
    D.cc[0] = wm == 2 ? 128 : 43; D.cc[1] = wm == 2 ? 1 : 3;
    {
      u32x4 sb2 = D.sb; sb2[2] = wm == 2 ? sb2[2] : 0u;
#pragma unroll
      for (int t = 1; t < 3; ++t) { pfetch(D.sa, lb + K1_PF + wm * 512, v0, t * D.stab); pfetch(sb2, lb + K1_PF + wm * 512 + 256, v1, wm == 2 ? t * D.stc : 0u); }
    }
    k1_body<1>(wsum, t1, acc, lb, wm, lane, sx, v0, v1, 0u, 0u, 0u, D);
    ST(t2 = mt_();)
    EPI_LANE();
    const int grow = b * 8 + 2 * wm + (lane >> 5);
    u32x4 gx[4], gw[4];
#pragma unroll
    for (int i = 0; i < 4; ++i) {
      gx[i] = *(const u32x4*)(x + grow * DM + i * 256 + (lane & 31) * 8);
      gw[i] = *(const u32x4*)(gatew + i * 256 + (lane & 31) * 8);
    }
#pragma unroll
    for (int mt = 0; mt < 4; ++mt) ST16(pack8(acc[mt][0], acc[mt][1]), WS_QF + (c * 128 + m16 + mt) * 1024);
    const int j0 = 7 * c + ((c & 1) ? 3 : 0);
    const unsigned hp = WS_AP + ((j0 >> 1) * 128 + m16) * 1024;
#pragma unroll
    for (int mt = 0; mt < 4; ++mt) {
      const f32x4 h0 = geglu4(acc[mt][2], acc[mt][6]), h1 = geglu4(acc[mt][3], acc[mt][7]);
      ST16(pack8(h0, h1), hp + mt * 1024);
      const f32x4 h2 = geglu4(acc[mt][4], acc[mt][8]), h3 = geglu4(acc[mt][5], acc[mt][9]);
      ST16(pack8(h2, h3), hp + 131072 + mt * 1024);
    }
    {
      float s = 0.f;
#pragma unroll
      for (int i = 0; i < 4; ++i)
#pragma unroll
        for (int e = 0; e < 4; ++e) {
          const unsigned pr = pk2(__uint_as_float(gx[i][e] << 16) * __uint_as_float(gw[i][e] << 16), __uint_as_float(gx[i][e] & 0xffff0000u) * __uint_as_float(gw[i][e] & 0xffff0000u));
          s += __uint_as_float(pr << 16) + __uint_as_float(pr & 0xffff0000u);
        }
#pragma unroll
      for (int o = 1; o < 32; o <<= 1) s += __shfl_xor(s, o);
      s = bfr(s);
      const float gv = bfr(1.0f / (1.0f + __expf(-s)));
      if ((lane & 31) == 0) ((float*)(ws + WS_G))[grow] = gv;
      if (wm == 0 && lane == 0) { ((unsigned*)(ws + WS_FLAGS))[(8 + b) * 16] = 0u; if (b < 8) ((unsigned*)(ws + WS_FLAGS))[b * 16] = 0u; }
    }
  }
  LGKM_BAR();
  {
    int tid = w * 64 + __builtin_amdgcn_mbcnt_hi(~0u, __builtin_amdgcn_mbcnt_lo(~0u, 0u)); asm volatile("" : "+v"(tid));
    const unsigned rd = lb + 65536 + tid * 16;
    const f32x4 s = *(LAS f32x4*)(rd) + *(LAS f32x4*)(rd + 8192) + *(LAS f32x4*)(rd + 16384) + *(LAS f32x4*)(rd + 24576);
    __builtin_amdgcn_raw_buffer_store_b64((u32x2){pk2(s[0], s[1]), pk2(s[2], s[3])}, rw, WS_KVP + ((c * 8 + w) * 4 + (rb >> 1)) * 1024 + (tid & 63) * 16 + (rb & 1) * 8, 0, ST_AUX);
    if (tid < 16) {
      const unsigned rz = lb + 65536 + 32768 + tid * 16;
      const f32x4 zs = *(LAS f32x4*)(rz) + *(LAS f32x4*)(rz + 256) + *(LAS f32x4*)(rz + 512) + *(LAS f32x4*)(rz + 768);
      *(f32x4*)(ws + WS_ZP + ((size_t)(c * 8 + rb) * 16 + tid) * 16) = zs;
    }
  }
}

constexpr unsigned K2_SLOT = 16384, K2_KV = 131072, K2_LDS = 131072 + 4608;
constexpr int NS = 18, NH = 14;
#define PSTR 65536u
#define PAUX 16
#define OUT_AUX 16
#define VM_BAR(N) do { asm volatile("" ::: "memory"); __builtin_amdgcn_s_waitcnt(((N) & 15) | 0xF70 | (((N) >> 4) << 14)); __builtin_amdgcn_s_barrier(); asm volatile("" ::: "memory"); } while (0)
#define BAR() do { asm volatile("" ::: "memory"); __builtin_amdgcn_s_barrier(); asm volatile("" ::: "memory"); } while (0)

DI void wait_ge(const unsigned* flag, unsigned need) {
  for (unsigned spin = 0; spin < (1u << 22); ++spin) {
    if (__hip_atomic_load((const GAS unsigned*)flag, __ATOMIC_RELAXED, __HIP_MEMORY_SCOPE_AGENT) >= need) break;
    __builtin_amdgcn_s_sleep(1);
  }
}
#define lda(d, srd, voff, soff) d = __builtin_bit_cast(bf16x8, __builtin_amdgcn_raw_buffer_load_b128(srd, voff, soff, 16))

__global__ __launch_bounds__(768, 3) void k2_kernel(const uint16_t* __restrict__ wdown, const uint16_t* __restrict__ wo, char* __restrict__ ws,
                                                    uint16_t* __restrict__ out) {
  extern __shared__ __attribute__((aligned(16))) char smem[];
  const unsigned lb = (unsigned)(size_t)(LAS char*)smem;
  const int lane = threadIdx.x & 63;
  const int w = __builtin_amdgcn_readfirstlane(threadIdx.x >> 6);
  const int g = lane >> 4;
  const int b = blockIdx.x, xj = b & 7, bi = b >> 3;
  const int kq = xj >> 1;
  const int mt = 4 * (xj & 1) + (bi >> 3), nt128 = bi & 7;
  const int tile = mt * 8 + nt128;
  const int oc = 4 * xj + (bi & 3), orb = bi >> 2;
  unsigned* const flags = (unsigned*)(ws + WS_FLAGS);
  const float* const gs = (const float*)(ws + WS_G);
  auto tix = [&](int s) -> int { return s < NH ? NH * kq + s : 56 - NH + 4 * kq + s; };

  if (w >= 8) {
    const int sw = w - 8;
    const u32x4 sd = mk_srd(wdown, DF * DM * 2), so_ = mk_srd(wo, DM * DM * 2);
    const unsigned vb0 = (16 * sw + g) * 2048 + nt128 * 256 + (((lane & 15) ^ (g << 1)) << 4), vb1 = (vb0 ^ 128) + 4 * 2048;
    auto stage = [&](int s) {
      if (s < NS) {
        u32x4 r;
#pragma unroll
        for (int j = 0; j < 4; ++j) r[j] = s >= NH ? so_[j] : sd[j];
        const unsigned so = (s >= NH ? 4 * kq + s - NH : NH * kq + s) * 131072, dst = lb + (s & 7) * K2_SLOT + sw * 4096;
#pragma unroll
        for (int i = 0; i < 4; ++i) if (ABL != 12) dma16(r, dst + i * 1024, (i & 1) ? vb1 : vb0, so + (i >> 1) * 16384);
      }
    };
    stage(0); stage(1); stage(2); stage(3);
    VM_BAR(8);
    for (int k = 0; k < NS / 2 - 1; ++k) {
      stage(2 * k + 4); stage(2 * k + 5);
      if (k == 2 && threadIdx.x == 512)
        __hip_atomic_fetch_add((GAS unsigned*)(flags + orb * 16), 1u, __ATOMIC_RELAXED, __HIP_MEMORY_SCOPE_AGENT);
      if (k == 5 && sw == 3) {
        if (lane == 0) wait_ge(flags + mt * 16, 32u);
        asm volatile("s_waitcnt vmcnt(0)" ::: "memory");
      }
      if (2 * k + 4 < NS) VM_BAR(8); else VM_BAR(0);
    }
    BAR();
    if (sw == 0) {
      if (lane == 0) __hip_atomic_store((GAS unsigned*)(flags + (8 + tile * 4 + kq) * 16), 1u, __ATOMIC_RELAXED, __HIP_MEMORY_SCOPE_AGENT);
      if (lane < 4 && lane != kq) wait_ge(flags + (8 + tile * 4 + lane) * 16, 1u);
      asm volatile("s_waitcnt vmcnt(0)" ::: "memory");
      BAR();
    }
    return;
  }

  auto compute = [&](auto grp_) {
  constexpr int GRP = decltype(grp_)::value;
  ST(unsigned long long wsum = 0, r0 = rt_(), t0 = mt_();)
  const int q = (lane & 15) >> 2, p = lane & 3;
  const auto sa = __builtin_amdgcn_make_buffer_rsrc((void*)(ws + WS_AP), 0, 144u * 131072u, 0x00020000);
  const unsigned va = lane * 16;
  const unsigned abase = (mt * 16 + 2 * w) * 1024;
  const bf16x8 wp0 = *(const bf16x8*)(ws + WS_WPF + oc * 2048 + va), wp1 = *(const bf16x8*)(ws + WS_WPF + oc * 2048 + 1024 + va);
  bf16x8 qf[2]; float go[2]; u32x4 kvl[4];
#pragma unroll
  for (int i = 0; i < 2; ++i) {
    qf[i] = *(const bf16x8*)(ws + WS_QF + ((size_t)oc * 128 + orb * 16 + 2 * w + i) * 1024 + va);
    go[i] = gs[(orb * 16 + 2 * w + i) * 16 + (lane & 15)];
  }
#pragma unroll
  for (int r = 0; r < 4; ++r) kvl[r] = *(const u32x4*)(ws + WS_KVP + (size_t)(oc * 8 + w) * 4096 + r * 1024 + va);
  const char* zp = ws + WS_ZP + ((size_t)oc * 8 * 16 + 2 * g * 16 + (lane & 15)) * 16;
  const f32x4 za_ = *(const f32x4*)zp, zb_ = *(const f32x4*)(zp + 256);
  asm volatile("" ::: "memory");
  bf16x8 ar[4][2];
  { const unsigned a0 = abase + tix(0) * 262144, a1 = abase + tix(1) * 262144;
    lda(ar[0][0], sa, va, a0); lda(ar[0][1], sa, va, a0 + 1024);
    lda(ar[1][0], sa, va, a0 + 131072); lda(ar[1][1], sa, va, a0 + 131072 + 1024);
    lda(ar[2][0], sa, va, a1); lda(ar[2][1], sa, va, a1 + 1024); }
  const float gsc0 = gs[mt * 256 + w * 32 + (lane & 15)], gsc1 = gs[mt * 256 + w * 32 + 16 + (lane & 15)];
  asm volatile("" ::: "memory");
  bf16x8 pf[2][2];
#pragma unroll
  for (int i = 0; i < 2; ++i) { f32x4 pp0, pp1, pn0, pn1; featmap(qf[i], wp0, wp1, pp0, pp1, pn0, pn1); pf[i][0] = pack8(pp0, pp1); pf[i][1] = pack8(pn0, pn1); }
  {
    f32x4 kvs = {0.f, 0.f, 0.f, 0.f};
#pragma unroll
    for (int r = 0; r < 4; ++r) kvs += unpk_lo(kvl[r]) + unpk_hi(kvl[r]);
    *(LAS u32x2*)(lb + K2_KV + ((w & 1) * 2 + (w >> 2)) * 1024 + va + ((w >> 1) & 1) * 8) = (u32x2){pk2(kvs[0], kvs[1]), pk2(kvs[2], kvs[3])};
    const f32x4 zs = za_ + zb_;
    float z0 = zs[0], z1 = zs[1], z2 = zs[2], z3 = zs[3]; XRED2(z0, z1, fadd_); XRED2(z2, z3, fadd_);
    if (w == 0) *(LAS u32x2*)(lb + K2_KV + 4096 + ((lane >> 3) & 1) * 64 + (lane & 3) * 16 + ((lane >> 2) & 1) * 8) = (u32x2){pk2(z0, z1), pk2(z2, z3)};
  }
  f32x4 acc[2][8];
#pragma unroll
  for (int m = 0; m < 2; ++m)
#pragma unroll
    for (int n = 0; n < 8; ++n) acc[m][n] = (f32x4){0.f, 0.f, 0.f, 0.f};
  const unsigned bb0 = lb + (4 * g + q) * 256 + (((4 * (g & 1) + q) << 1 | (p >> 1)) << 4) + (p & 1) * 8;
  unsigned ba[8];
#pragma unroll
  for (int n = 0; n < 8; ++n) ba[n] = bb0 ^ (n << 5);
  unsigned sp2 = 0;
  bf16x8 bw[8];
  auto ldB = [&](int par, int u) -> bf16x8 { const unsigned a = ba[u & 7] + par * K2_SLOT + (u >> 3) * 8192; return trrd2(a, a + 4096); };
  __builtin_amdgcn_s_waitcnt(0xC07F);
  BAR();
  {
    bf16x8 ka[2][2], za[2];
#pragma unroll
    for (int ks = 0; ks < 2; ++ks) {
      ka[0][ks] = ldsrd(lb + K2_KV + (0 + ks) * 1024 + va);
      ka[1][ks] = ldsrd(lb + K2_KV + (2 + ks) * 1024 + va);
      za[ks] = ldsrd(lb + K2_KV + 4096 + ks * 64 + g * 16);
    }
#pragma unroll
    for (int i = 0; i < 2; ++i) {
      const f32x4 z4 = {0.f, 0.f, 0.f, 0.f};
      f32x4 n0 = mfma16(ka[0][1], pf[i][1], mfma16(ka[0][0], pf[i][0], z4));
      f32x4 n1 = mfma16(ka[1][1], pf[i][1], mfma16(ka[1][0], pf[i][0], z4));
      f32x4 dn = mfma16(za[1], pf[i][1], mfma16(za[0], pf[i][0], z4));
      const float sc = go[i] * __builtin_amdgcn_rcpf(dn[0] + 1e-6f);
      n0 *= sc; n1 *= sc;
      __builtin_amdgcn_raw_buffer_store_b128(__builtin_bit_cast(u32x4, pack8(n0, n1)), sa, va + ((112 + oc) * 128 + orb * 16 + 2 * w + i) * 1024, 0, 16  );
    }
    __builtin_amdgcn_s_waitcnt(0xC07F);
    asm volatile("" ::: "memory");
  }
  SB(); MFMA_FENCE();
  ST(unsigned long long t1 = mt_();)
#pragma unroll
  for (int u = 0; u < 6; ++u) bw[u] = ldB(0, u);
  const auto rp = __builtin_amdgcn_make_buffer_rsrc((void*)(ws + WS_PART), 0, 256u * PSTR, 0x00020000);
  const unsigned ex = lb + 32768;
  const bool own = (w >> 1) == kq;
  auto emit = [&](int n) {
    if (own) {
#pragma unroll
      for (int m = 0; m < 2; ++m) { *(LAS f32x4*)(ex + (((w & 1) * 2 + m) * 8 + 2 * n) * 1024 + va) = acc[m][2 * n]; *(LAS f32x4*)(ex + (((w & 1) * 2 + m) * 8 + 2 * n + 1) * 1024 + va) = acc[m][2 * n + 1]; }
    } else {
#pragma unroll
      for (int m = 0; m < 2; ++m)
        __builtin_amdgcn_raw_buffer_store_b128(__builtin_bit_cast(u32x4, pack8(acc[m][2 * n], acc[m][2 * n + 1])), rp, va + (tile * 4 + kq) * PSTR + w * 8192 + m * 4096 + n * 1024, 0, PAUX);
    }
  };
  auto ldF = [&](int v) -> bf16x8 { const int blk = (v >> 2) & 3; return ldB(blk >> 1, (blk & 1) * 8 + 4 * (v >> 4) + (v & 3)); };
  auto step = [&](int s, auto par_, auto fin_) {
    constexpr int par = decltype(par_)::value;
    constexpr bool ia0 = true, ia1 = true, nxt = true, fin = decltype(fin_)::value;
    const unsigned an1 = abase + tix(s + 1) * 262144 + 131072, an2 = abase + tix(s + 2) * 262144;
#pragma unroll
    for (int u = 0; u < 16; ++u) {
      if (u == 10 && nxt && par == 1) {
        VM_BAR(8);
        const unsigned nx = (sp2 + 32768) & 98304, dx = sp2 ^ nx; sp2 = nx;
#pragma unroll
        for (int n = 0; n < 8; ++n) { ba[n] ^= dx; asm volatile("" : "+v"(ba[n])); }
      }
      if (u < 10) bw[(u + 6) & 7] = ldB(par, u + 6);
      else bw[(u + 6) & 7] = fin ? ldF(u - 10) : ldB(par ^ 1, u - 10);
      if (ia0 && u < 2) lda(ar[(2 * par + 3) & 3][u], sa, va, an1 + u * 1024);
      if (ia1 && (u == 8 || u == 9)) lda(ar[2 * par][u - 8], sa, va, an2 + (u - 8) * 1024);
      if ((u & 1) == 0 && (u < 10 || nxt)) __builtin_amdgcn_s_waitcnt(0xCA7F);
#pragma unroll
      for (int m = 0; m < 2; ++m) mfma_ip(acc[m][u & 7], bw[u & 7], ar[2 * par + (u >> 3)][m]);
      SB();
    }
  };
#pragma unroll 1
  for (int s = 0; s < NH; s += 2) { step(s, I0{}, I0{}); step(s + 1, I1{}, I0{}); }
  {
    MFMA_FENCE(); SB();
    const float s0 = bfr(1.0f - gsc0), s1 = bfr(1.0f - gsc1);
#pragma unroll
    for (int n = 0; n < 8; ++n) { acc[0][n] *= s0; acc[1][n] *= s1; }
    SB(); MFMA_FENCE();
  }
  step(NH, I0{}, I0{}); step(NH + 1, I1{}, I1{});
  ST(wsum = mt_();)
  {
    const unsigned an1 = abase + tix(NS - 1) * 262144 + 131072;
#pragma unroll
    for (int v = 0; v < 32; ++v) {
      if (v + 6 < 32) bw[(v + 6) & 7] = ldF(v + 6);
      if (v < 2) lda(ar[3][v], sa, va, an1 + v * 1024);
      if ((v & 1) == 0 && v + 6 < 32) __builtin_amdgcn_s_waitcnt(0xCA7F);
#pragma unroll
      for (int m = 0; m < 2; ++m) mfma_ip(acc[m][4 * (v >> 4) + (v & 3)], bw[v & 7], ar[(v >> 2) & 3][m]);
      SB();
      if (v == 17) { emit(0); emit(1); SB(); }
    }
  }
  MFMA_FENCE(); SB();
  ST(unsigned long long t2 = mt_();)
  emit(2); emit(3);
  ST(unsigned long long e0 = mt_();)
  WAIT_BAR(0);
  ST(unsigned long long e1 = mt_();)
  f32x4 fin[4];
#pragma unroll
  for (int n = 0; n < 4; ++n) fin[n] = *(LAS f32x4*)(ex + ((w >> 1) * 8 + 4 * (w & 1) + n) * 1024 + va);
  BAR();
  ST(unsigned long long e2 = mt_();)
  {
    u32x4 part[3][2];
#pragma unroll
    for (int j = 0; j < 3; ++j) {
      const int src = (kq + 1 + j) & 3;
      const unsigned po = (tile * 4 + src) * PSTR + (2 * kq + (w >> 2)) * 8192 + ((w >> 1) & 1) * 4096 + 2 * (w & 1) * 1024;
      part[j][0] = __builtin_amdgcn_raw_buffer_load_b128(rp, va, po, 16);
      part[j][1] = __builtin_amdgcn_raw_buffer_load_b128(rp, va, po + 1024, 16);
    }
    const auto ro = __builtin_amdgcn_make_buffer_rsrc((void*)out, 0, SEQ * DM * 2, 0x00020000);
    const unsigned oo = ((mt * 256 + kq * 64 + (w >> 1) * 16 + (lane & 15)) * DM + nt128 * 128 + (w & 1) * 64 + (g & 1) * 16 + (g >> 1) * 8) * 2;
#pragma unroll
    for (int n = 0; n < 2; ++n) {
      const f32x4 v0 = fin[2 * n] + (unpk_lo(part[0][n]) + unpk_lo(part[1][n]) + unpk_lo(part[2][n]));
      const f32x4 v1 = fin[2 * n + 1] + (unpk_hi(part[0][n]) + unpk_hi(part[1][n]) + unpk_hi(part[2][n]));
      const auto r0 = __builtin_amdgcn_permlane16_swap(pk2(v0[0], v0[1]), pk2(v1[0], v1[1]), false, false);
      const auto r1 = __builtin_amdgcn_permlane16_swap(pk2(v0[2], v0[3]), pk2(v1[2], v1[3]), false, false);
      __builtin_amdgcn_raw_buffer_store_b128((u32x4){r0[0], r1[0], r0[1], r1[1]}, ro, oo + n * 64, 0, OUT_AUX);
    }
  }
  };
  compute(I0{});
}

extern "C" void kernel_launch(void* const* d_in, const int* in_sizes, int n_in, void* d_out, int out_size, void* d_ws, size_t ws_size,
                              hipStream_t stream) {
  const uint16_t* x = (const uint16_t*)d_in[0];
  const uint16_t* wqkv = (const uint16_t*)d_in[1];
  const uint16_t* wphi = (const uint16_t*)d_in[2];
  const uint16_t* wo = (const uint16_t*)d_in[3];
  const uint16_t* wg = (const uint16_t*)d_in[5];
  const uint16_t* wu = (const uint16_t*)d_in[6];
  const uint16_t* wd = (const uint16_t*)d_in[7];
  const uint16_t* gw = (const uint16_t*)d_in[8];
  (void)hipFuncSetAttribute((const void*)k1_kernel, hipFuncAttributeMaxDynamicSharedMemorySize, K1_LDS);
  (void)hipFuncSetAttribute((const void*)k2_kernel, hipFuncAttributeMaxDynamicSharedMemorySize, K2_LDS);
  k1_kernel<<<256, 512, K1_LDS, stream>>>(x, wqkv, wphi, wg, wu, gw, wd, wo, (char*)d_ws);
  k2_kernel<<<256, 768, K2_LDS, stream>>>(wd, wo, (char*)d_ws, (uint16_t*)d_out);
}
```

```cpp
#include <hip/hip_runtime.h>
#include <hip/hip_bf16.h>
#include <stdint.h>
#include <type_traits>
#pragma clang diagnostic ignored "-Wint-to-pointer-cast"

typedef short bf16x8 __attribute__((ext_vector_type(8)));
typedef short s16x4 __attribute__((ext_vector_type(4)));
typedef float f32x4 __attribute__((ext_vector_type(4)));
typedef float f32x2 __attribute__((ext_vector_type(2)));
typedef unsigned u32x4 __attribute__((ext_vector_type(4)));
typedef unsigned u32x2 __attribute__((ext_vector_type(2)));
typedef __bf16 bf16x2_t __attribute__((ext_vector_type(2)));
#define LAS __attribute__((address_space(3)))
#define GAS __attribute__((address_space(1)))
#define DI __device__ __forceinline__

constexpr int SEQ = 2048, DM = 1024, DF = 3584;
constexpr size_t WS_FLAGS = 0;
constexpr size_t WS_G     = 32768;
constexpr size_t WS_ZP    = 65536;
constexpr size_t WS_KVP   = 262144;
constexpr size_t WS_WPF   = 3u << 20;
constexpr size_t WS_QF    = 4u << 20;
constexpr size_t WS_AP    = 16u << 20;
constexpr size_t WS_PART  = 40u << 20;

DI f32x4 mfma16(bf16x8 a, bf16x8 b, f32x4 c) { return __builtin_amdgcn_mfma_f32_16x16x32_bf16(a, b, c, 0, 0, 0); }
DI void mfma_ip(f32x4& c, bf16x8 a, bf16x8 b) { asm volatile("v_mfma_f32_16x16x32_bf16 %0, %1, %2, %0" : "+v"(c) : "v"(a), "v"(b)); }
#define MFMA_FENCE() asm volatile("s_nop 7\n\ts_nop 7" ::: "memory")
DI s16x4 trrd(unsigned a) { return __builtin_amdgcn_ds_read_tr16_b64_v4i16((LAS s16x4*)a); }
DI bf16x8 trrd2(unsigned a0, unsigned a1) { s16x4 lo = trrd(a0), hi = trrd(a1); return __builtin_shufflevector(lo, hi, 0, 1, 2, 3, 4, 5, 6, 7); }
DI bf16x8 ldsrd(unsigned a) { return *(LAS bf16x8*)a; }
DI unsigned pk2(float lo, float hi) { f32x2 v = {lo, hi}; bf16x2_t b = __builtin_convertvector(v, bf16x2_t); return __builtin_bit_cast(unsigned, b); }
DI bf16x8 pack8(f32x4 a, f32x4 b) { u32x4 r = {pk2(a[0], a[1]), pk2(a[2], a[3]), pk2(b[0], b[1]), pk2(b[2], b[3])}; return __builtin_bit_cast(bf16x8, r); }
DI f32x4 unpk_lo(u32x4 v) { return (f32x4){__uint_as_float(v[0] << 16), __uint_as_float(v[0] & 0xffff0000u), __uint_as_float(v[1] << 16), __uint_as_float(v[1] & 0xffff0000u)}; }
DI f32x4 unpk_hi(u32x4 v) { return (f32x4){__uint_as_float(v[2] << 16), __uint_as_float(v[2] & 0xffff0000u), __uint_as_float(v[3] << 16), __uint_as_float(v[3] & 0xffff0000u)}; }
DI float bfr(float x) { unsigned u = pk2(x, x); return __uint_as_float(u << 16); }
DI u32x4 mk_srd(const void* p, unsigned bytes) {
  uint64_t a = (uint64_t)p;
  u32x4 r;
  r[0] = __builtin_amdgcn_readfirstlane((unsigned)a);
  r[1] = __builtin_amdgcn_readfirstlane((unsigned)(a >> 32) & 0xffffu);
  r[2] = __builtin_amdgcn_readfirstlane(bytes);
  r[3] = 0x00020000u;
  return r;
}
DI void pfetch(u32x4 srd, unsigned lds, unsigned voff, unsigned soff) {
  asm volatile("s_mov_b32 m0, %0\n\ts_nop 0\n\tbuffer_load_dword %1, %2, %3 offen lds" ::"s"(lds), "v"(voff), "s"(srd), "s"(soff) : "memory");
}
DI void dma16(u32x4 srd, unsigned lds, unsigned voff, unsigned soff) {
  asm volatile("s_mov_b32 m0, %0\n\ts_nop 0\n\tbuffer_load_dwordx4 %1, %2, %3 offen lds" ::"s"(lds), "v"(voff), "s"(srd), "s"(soff) : "memory");
}
#define WAIT_BAR(N) do { asm volatile("" ::: "memory"); __builtin_amdgcn_s_waitcnt(((N) & 15) | 0x70 | (((N) >> 4) << 14)); __builtin_amdgcn_s_barrier(); asm volatile("" ::: "memory"); } while (0)
#define LGKM_BAR() do { asm volatile("" ::: "memory"); __builtin_amdgcn_s_waitcnt(0xC07F); __builtin_amdgcn_s_barrier(); asm volatile("" ::: "memory"); } while (0)
#define SB() __builtin_amdgcn_sched_barrier(0)

DI float fmx(float a, float b) { return __builtin_amdgcn_fmed3f(a, b, __builtin_inff()); }
#define XRED2(a, b, OP) do { \
    auto r1_ = __builtin_amdgcn_permlane16_swap(__float_as_uint(a), __float_as_uint(b), false, false); \
    float m_ = OP(__uint_as_float(r1_[0]), __uint_as_float(r1_[1])); \
    auto r2_ = __builtin_amdgcn_permlane32_swap(__float_as_uint(m_), __float_as_uint(m_), false, false); \
    m_ = OP(__uint_as_float(r2_[0]), __uint_as_float(r2_[1])); \
    auto r3_ = __builtin_amdgcn_permlane16_swap(__float_as_uint(m_), __float_as_uint(m_), false, false); \
    a = __uint_as_float(r3_[0]); b = __uint_as_float(r3_[1]); } while (0)
DI float fadd_(float a, float b) { return a + b; }
DI f32x4 exp2v(f32x4 v) { return (f32x4){__builtin_amdgcn_exp2f(v[0]), __builtin_amdgcn_exp2f(v[1]), __builtin_amdgcn_exp2f(v[2]), __builtin_amdgcn_exp2f(v[3])}; }

DI void featmap(bf16x8 fr, bf16x8 wp0, bf16x8 wp1, f32x4& pp0, f32x4& pp1, f32x4& pn0, f32x4& pn1) {
  const f32x4 z4 = {0.f, 0.f, 0.f, 0.f};
  constexpr float L2E = 1.4426950408889634f;
  const f32x4 t0 = mfma16(wp0, fr, z4) * L2E, t1 = mfma16(wp1, fr, z4) * L2E;
  float mx = fmaxf(fmaxf(fmaxf(t0[0], t0[1]), fmaxf(t0[2], t0[3])), fmaxf(fmaxf(t1[0], t1[1]), fmaxf(t1[2], t1[3])));
  float nm = -fminf(fminf(fminf(t0[0], t0[1]), fminf(t0[2], t0[3])), fminf(fminf(t1[0], t1[1]), fminf(t1[2], t1[3])));
  XRED2(mx, nm, fmx);
  pp0 = exp2v(t0 - mx); pp1 = exp2v(t1 - mx);
  pn0 = exp2v(-nm - t0); pn1 = exp2v(-nm - t1);
  const f32x4 s4p = pp0 + pp1, s4n = pn0 + pn1;
  float sp = (s4p[0] + s4p[1]) + (s4p[2] + s4p[3]), sn = (s4n[0] + s4n[1]) + (s4n[2] + s4n[3]);
  XRED2(sp, sn, fadd_);
  const float ip = __builtin_amdgcn_rcpf(sp), in = __builtin_amdgcn_rcpf(sn);
  pp0 *= ip; pp1 *= ip; pn0 *= in; pn1 *= in;
}

DI f32x4 geglu4(f32x4 g, f32x4 u) {
  constexpr float C1 = -2.0f * 0.7978845608028654f * 1.4426950408889634f, C2 = C1 * 0.044715f;
  const f32x4 d = exp2v(g * (g * g * C2 + C1)) + 1.0f;
  const f32x4 r = {__builtin_amdgcn_rcpf(d[0]), __builtin_amdgcn_rcpf(d[1]), __builtin_amdgcn_rcpf(d[2]), __builtin_amdgcn_rcpf(d[3])};
  return (g * u) * r;
}
constexpr int ST_AUX = 16;
using I0 = std::integral_constant<int, 0>; using I1 = std::integral_constant<int, 1>;

constexpr unsigned K1_X0 = 0, K1_XS = 32768, K1_W0 = 65536, K1_WS = 40960, K1_WG = 12288, K1_WU = 26624, K1_PHI = 147456, K1_PF = 149504, K1_LDS = 151552;

constexpr int WR = 5, PFU = 4;
struct K1Dma {
  u32x4 sa, sb, sc;
  unsigned co[4], cc[2];
  unsigned lo[10];
  unsigned stab, stc;
};

template <int WN>
DI void k1_body(f32x4 (&acc)[4][10], const unsigned lb, const int wm, const int lane,
                const u32x4 sx, const unsigned v0, const unsigned v1, const unsigned v2, const unsigned vxa, const unsigned vxb, const K1Dma& D) {
  const int g = lane >> 4, q = (lane & 15) >> 2, p = lane & 3, fr = lane & 15;
  const unsigned xrow = wm * 64 + fr;
  const unsigned xb0 = lb + K1_X0 + xrow * 128 + ((g ^ ((xrow >> 1) & 7)) << 4);
  const unsigned xb1 = xb0 ^ 64;
  const unsigned qb0 = lb + K1_W0 + (8 * g + q) * 192 + ((0 ^ (g & 1)) << 5) + p * 8;
  const unsigned qb1 = lb + K1_W0 + (8 * g + q) * 192 + ((1 ^ (g & 1)) << 5) + p * 8;
  const unsigned gb0 = lb + K1_W0 + K1_WG + (8 * g + 4 * (0 ^ (g & 1)) + q) * 224 + p * 8;
  const unsigned gb1 = lb + K1_W0 + K1_WG + (8 * g + 4 * (1 ^ (g & 1)) + q) * 224 + p * 8;
  const unsigned xl = lb + K1_X0 + wm * 8192, wl = lb + K1_W0;
  unsigned sxo = 128, tab = D.stab, tc = D.stc;
  int pt = 3;

  bf16x8 xf[2][4], wf[WR];
  auto ldW = [&](int st, int u) -> bf16x8 {
    const int ks = u / 10, i = u % 10;
    const unsigned so = st * K1_WS;
    constexpr int NQ = WN == 0 ? 4 : 2;
    constexpr int NG = WN == 0 ? 3 : 4;
    constexpr int P0 = WN == 0 ? 4 : 0;
    if (i < NQ) {
      const int ptl = WN == 0 ? 1 + (i >> 1) : 0, nh = i & 1;
      const unsigned b = (nh ? qb1 : qb0) + so + ks * 32 * 192 + ptl * 64;
      return trrd2(b, b + 4 * 192);
    } else {
      const int j = i - NQ, up = j / NG, P = P0 + j % NG;
      const unsigned o = so + ks * 32 * 224 + up * (K1_WU - K1_WG) + P * 32;
      return trrd2(gb0 + o, gb1 + o);
    }
  };
  auto ldX = [&](int st, int ks, int mt) -> bf16x8 { return ldsrd((ks ? xb1 : xb0) + st * K1_XS + mt * 2048); };
  auto mma = [&](int st, int u) {
    const int ks = u / 10, i = u % 10, ri = (st * 20 + u) % WR;
#pragma unroll
    for (int mt = 0; mt < 4; ++mt) {
      if (WN == 0 && (i == 2 || i == 3)) mfma_ip(acc[mt][i], xf[ks][mt], wf[ri]);
      else mfma_ip(acc[mt][i], wf[ri], xf[ks][mt]);
    }
  };
  auto dmaX = [&](int st, int j) { dma16(sx, xl + st * K1_XS + j * 1024, (j & 1) ? vxb : vxa, sxo + j * 16384); };
  auto dmaW = [&](int st, int s) {
    if (s < 8) dma16((s & 2) ? D.sb : D.sa, wl + st * K1_WS + D.lo[s], s < 4 ? v0 : v1, tab + D.co[s & 3]);
    else dma16(D.sc, wl + st * K1_WS + D.lo[s], v2, tc + D.cc[s - 8]);
  };
  auto warm = [&](int k) {
    const unsigned i0 = (unsigned)(pt - (int)D.co[2 * k]), i = k ? (i0 * D.cc[0]) >> 7 : i0;
    const bool on = i <= D.co[2 * k + 1] && (!k || i * D.cc[1] == i0);
    u32x4 r = k ? D.sb : D.sa;
    r[2] = on ? r[2] : 0u;
    pfetch(r, lb + K1_PF + wm * 512 + k * 256, k ? v1 : v0, (on ? i : 0u) * (k ? D.stc : D.stab));
  };

  constexpr int WD = WR - 1, UB = 20 - WD;
  if (WN == 0) {
#pragma unroll
    for (int sl = 0; sl < 19 - UB; ++sl) {
      if (sl < 5) { dmaW(1, 2 * sl); dmaW(1, 2 * sl + 1); } else { dmaX(1, 2 * (sl - 5)); dmaX(1, 2 * (sl - 5) + 1); }
    }
    WAIT_BAR(2 * (19 - UB));
  } else WAIT_BAR(4);
#pragma unroll
  for (int mt = 0; mt < 4; ++mt) xf[0][mt] = ldX(0, 0, mt);
#pragma unroll
  for (int u = 0; u < WD; ++u) wf[u] = ldW(0, u);
  SB(); MFMA_FENCE();

  auto step = [&](auto st_, auto dox_, auto dow_) {
    constexpr int st = decltype(st_)::value;
    constexpr bool dox = decltype(dox_)::value, dow = decltype(dow_)::value;
#pragma unroll
    for (int u = 0; u < 20; ++u) {
      if (u == UB + 1) {
        if (WN == 0) { sxo += 128; tab += D.stab; tc += D.stc; } else ++pt;
        if (WN == 0) WAIT_BAR(0); else WAIT_BAR(63);
        if (dox) wf[(st * 20 + UB + WD) % WR] = ldW(st ^ 1, 0);
      }
      if (u < UB) wf[(st * 20 + u + WD) % WR] = ldW(st, u + WD);
      else if (u > UB && dox) wf[(st * 20 + u + WD) % WR] = ldW(st ^ 1, u - UB);
      if (u >= 2 && u < 6) xf[1][u - 2] = ldX(st, 1, u - 2);
      if (u >= 16 && u > UB && dox) { if (UB >= 16) { if (u == UB + 1) { for (int mt = 0; mt < 4; ++mt) xf[0][mt] = ldX(st ^ 1, 0, mt); } } else xf[0][u - 16] = ldX(st ^ 1, 0, u - 16); }
      if (WN == 0) {
        const int sl = u > UB ? u - UB - 1 : u + 19 - UB;
        const bool post = u > UB;
        if (sl < 5) { if (post ? dow : dox) { dmaW(post ? st : st ^ 1, 2 * sl); dmaW(post ? st : st ^ 1, 2 * sl + 1); } }
        else if (sl < 9) { if (post ? dow : dox) { dmaX(post ? st : st ^ 1, 2 * (sl - 5)); dmaX(post ? st : st ^ 1, 2 * (sl - 5) + 1); } }
      }
      if (WN == 1 && dow && (u == PFU || u == PFU + 2)) warm(u != PFU);
      mma(st, u);
      SB();
    }
  };
  for (int t2 = 0; t2 < 7; ++t2) { step(I0{}, I1{}, I1{}); step(I1{}, I1{}, I1{}); }
  step(I0{}, I1{}, I0{});
  step(I1{}, I0{}, I0{});
  MFMA_FENCE(); SB();
}

__global__ __launch_bounds__(512, 2) void k1_kernel(const uint16_t* __restrict__ x, const uint16_t* __restrict__ wqkv, const uint16_t* __restrict__ wphi,
                                                    const uint16_t* __restrict__ wg, const uint16_t* __restrict__ wu, const uint16_t* __restrict__ gatew,
                                                    const uint16_t* __restrict__ wdown, const uint16_t* __restrict__ wo, char* __restrict__ ws) {
  extern __shared__ __attribute__((aligned(16))) char smem[];
  const unsigned lb = (unsigned)(size_t)(LAS char*)smem;
  const int tid = threadIdx.x, lane = tid & 63;
  const int w = __builtin_amdgcn_readfirstlane(tid >> 6);
  const int wn = w >> 2, wm = w & 3;
  const int b = blockIdx.x, xj = b & 7, bi = b >> 3;
  const int c = 4 * xj + (bi & 3), rb = bi >> 2;
  const u32x4 sx = mk_srd(x, SEQ * DM * 2);
  const u32x4 sq = mk_srd(wqkv, DM * 3072 * 2), sg = mk_srd(wg, DM * DF * 2), su = mk_srd(wu, DM * DF * 2);
  f32x4 acc[4][10];
#pragma unroll
  for (int m = 0; m < 4; ++m)
#pragma unroll
    for (int i = 0; i < 10; ++i) acc[m][i] = (f32x4){0.f, 0.f, 0.f, 0.f};
  K1Dma D;
  const int m16 = rb * 16 + wm * 4;
  const auto rw = __builtin_amdgcn_make_buffer_rsrc((void*)ws, 0, 64u << 20, 0x00020000);
#define EPI_LANE() int lane = __builtin_amdgcn_mbcnt_hi(~0u, __builtin_amdgcn_mbcnt_lo(~0u, 0u)); asm volatile("" : "+v"(lane)); const int g = lane >> 4;
#define ST16(val, so) __builtin_amdgcn_raw_buffer_store_b128(__builtin_bit_cast(u32x4, val), rw, lane * 16 + (unsigned)(so), 0, ST_AUX)
  if (wn == 0) {
    const unsigned vxa = (rb * 256 + 64 * wm + (lane >> 3)) * 2048 + (((lane & 7) ^ (lane >> 4)) << 4), vxb = vxa ^ 64;
    const unsigned gtbl = (c & 1) ? 0x0216543u : 0x6543210u;
    auto pat = [&](bool isq, int pi) -> unsigned {
      const int ci = 64 * pi + lane, r = (ci * (isq ? 5462 : 4682)) >> 16, cc = ci - (isq ? 12 : 14) * r;
      const int o8 = (r >> 3) & 1;
      const unsigned vq = r * 6144 + ((cc >> 2) * 1024 + 32 * c + (((cc >> 1) & 1) ^ o8) * 16 + (cc & 1) * 8) * 2;
      const unsigned vg = (r ^ (o8 << 2)) * 7168 + (112 * c + 16 * ((gtbl >> (4 * (cc >> 1))) & 7) + 8 * (cc & 1)) * 2;
      return isq ? vq : vg;
    };
    const bool q01 = wm == 3, q2 = wm >= 2;
    const unsigned v0 = pat(q01, q01 ? 0 : 2 * wm), v1 = pat(q01, q01 ? 1 : 2 * wm + 1), v2 = pat(q2, q2 ? 2 : 6);
#pragma unroll
    for (int i = 0; i < 4; ++i) { D.sa[i] = q01 ? sq[i] : sg[i]; D.sb[i] = q01 ? sq[i] : su[i]; D.sc[i] = q2 ? sq[i] : (wm ? su[i] : sg[i]); }
    D.stab = q01 ? 64 * 6144 : 64 * 7168; D.stc = q2 ? 64 * 6144 : 64 * 7168;
    D.co[0] = 0; D.co[1] = q01 ? 16 * 6144 : 32 * 7168; D.co[2] = q01 ? 32 * 6144 : 0; D.co[3] = q01 ? 48 * 6144 : 32 * 7168;
#pragma unroll
    for (int h = 0; h < 2; ++h)
#pragma unroll
      for (int i = 0; i < 4; ++i) D.lo[4 * h + i] = q01 ? h * 1024 + i * 3072 : K1_WG + (2 * wm + h) * 1024 + i * 7168;
    D.cc[0] = q2 ? (wm - 2) * 32 * 6144 : 0; D.cc[1] = D.cc[0] + (q2 ? 16 * 6144 : 32 * 7168);
    D.lo[8] = q2 ? 2048 + (wm - 2) * 6144 : K1_WG + wm * 14336 + 6144; D.lo[9] = D.lo[8] + (q2 ? 3072 : 7168);
#pragma unroll
    for (int s = 0; s < 10; ++s) {
      if (s < 8) dma16((s & 2) ? D.sb : D.sa, lb + K1_W0 + D.lo[s], s < 4 ? v0 : v1, D.co[s & 3]);
      else dma16(D.sc, lb + K1_W0 + D.lo[s], v2, D.cc[s - 8]);
    }
    if (wm < 2) dma16(mk_srd(wphi, 32 * 32 * 32 * 2), lb + K1_PHI + wm * 1024, lane * 16, c * 2048 + wm * 1024);
    k1_body<0>(acc, lb, wm, lane, sx, v0, v1, v2, vxa, vxb, D);
    EPI_LANE();
    const int q = (lane & 15) >> 2, p = lane & 3;
    const unsigned wpb = lb + K1_PHI + (4 * g + q) * 64 + p * 8;
    const bf16x8 wp0 = trrd2(wpb, wpb + 16 * 64), wp1 = trrd2(wpb + 32, wpb + 16 * 64 + 32);
    if (rb == 0 && wm == 0) { ST16(wp0, WS_WPF + c * 2048); ST16(wp1, WS_WPF + c * 2048 + 1024); }
    const unsigned pkb = lb + wm * 8704;
    const unsigned pw = pkb + (lane & 15) * 136 + g * 8;
#pragma unroll
    for (int mt = 0; mt < 4; ++mt) {
      f32x4 pp0, pp1, pn0, pn1;
      featmap(pack8(acc[mt][0], acc[mt][1]), wp0, wp1, pp0, pp1, pn0, pn1);
      *(LAS u32x2*)(pw + mt * 16 * 136 + 0)  = (u32x2){pk2(pp0[0], pp0[1]), pk2(pp0[2], pp0[3])};
      *(LAS u32x2*)(pw + mt * 16 * 136 + 32) = (u32x2){pk2(pp1[0], pp1[1]), pk2(pp1[2], pp1[3])};
      *(LAS u32x2*)(pw + mt * 16 * 136 + 64) = (u32x2){pk2(pn0[0], pn0[1]), pk2(pn0[2], pn0[3])};
      *(LAS u32x2*)(pw + mt * 16 * 136 + 96) = (u32x2){pk2(pn1[0], pn1[1]), pk2(pn1[2], pn1[3])};
    }
    asm volatile("s_waitcnt lgkmcnt(0)" ::: "memory");
    f32x4 kv[4][2], zz[4];
#pragma unroll
    for (int ft = 0; ft < 4; ++ft) { kv[ft][0] = (f32x4){0.f, 0.f, 0.f, 0.f}; kv[ft][1] = kv[ft][0]; zz[ft] = kv[ft][0]; }
    const bf16x8 ones = {0x3f80, 0x3f80, 0x3f80, 0x3f80, 0x3f80, 0x3f80, 0x3f80, 0x3f80};
    const unsigned pr = pkb + (4 * g + q) * 136 + p * 8;
#pragma unroll
    for (int mp = 0; mp < 2; ++mp) {
      const bf16x8 va = pack8(acc[2 * mp][2], acc[2 * mp + 1][2]), vb = pack8(acc[2 * mp][3], acc[2 * mp + 1][3]);
#pragma unroll
      for (int ft = 0; ft < 4; ++ft) {
        const bf16x8 a = trrd2(pr + (32 * mp) * 136 + ft * 32, pr + (32 * mp + 16) * 136 + ft * 32);
        kv[ft][0] = mfma16(a, va, kv[ft][0]);
        kv[ft][1] = mfma16(a, vb, kv[ft][1]);
        zz[ft] = mfma16(a, ones, zz[ft]);
      }
    }
    const unsigned rd = lb + 65536 + wm * 8192 + lane * 16;
#pragma unroll
    for (int ft = 0; ft < 4; ++ft) {
      *(LAS f32x4*)(rd + (ft * 2 + 0) * 1024) = kv[ft][0];
      *(LAS f32x4*)(rd + (ft * 2 + 1) * 1024) = kv[ft][1];
    }
    { const int ft = lane & 3;
      const f32x4 zsel = ft == 0 ? zz[0] : (ft == 1 ? zz[1] : (ft == 2 ? zz[2] : zz[3]));
      *(LAS f32x4*)(lb + 65536 + 32768 + wm * 256 + ft * 64 + g * 16) = zsel; }
    const int jp = 7 * c + ((c & 1) ? 1 : 4), js = 7 * c + ((c & 1) ? 0 : 6);
    const unsigned hp = WS_AP + ((jp >> 1) * 128 + m16) * 1024, hs = WS_AP + ((js >> 1) * 128 + m16) * 1024 + (js & 1) * 8;
#pragma unroll
    for (int mt = 0; mt < 4; ++mt) {
      const f32x4 h0 = geglu4(acc[mt][4], acc[mt][7]), h1 = geglu4(acc[mt][5], acc[mt][8]), h2 = geglu4(acc[mt][6], acc[mt][9]);
      ST16(pack8(h0, h1), hp + mt * 1024);
      __builtin_amdgcn_raw_buffer_store_b64((u32x2){pk2(h2[0], h2[1]), pk2(h2[2], h2[3])}, rw, lane * 16 + hs + mt * 1024, 0, 0);
    }
  } else {
    {
      const unsigned vxa = (rb * 256 + 64 * wm + (lane >> 3)) * 2048 + (((lane & 7) ^ (lane >> 4)) << 4), vxb = vxa ^ 64;
#pragma unroll
      for (int j = 0; j < 8; ++j) dma16(sx, lb + K1_X0 + wm * 8192 + j * 1024, (j & 1) ? vxb : vxa, j * 16384);
    }
    const u32x4 sdn = mk_srd(wdown, DF * DM * 2), swo = mk_srd(wo, DM * DM * 2);
    const int r8 = 8 * rb + ((lane >> 2) & 7), l3 = lane & 3;
    const unsigned vx_ = (rb * 256 + (c & 3) * 64 + 32 * wm + (lane >> 1)) * 2048 + (lane & 1) * 64;
    const unsigned vg_ = r8 * 7168 + ((224 * c) & ~63) + l3 * 64;
    const unsigned vq_ = r8 * 6144 + (l3 == 3 ? 0 : l3) * 2048 + 64 * c;
    const unsigned v0 = wm < 2 ? vx_ : (wm == 2 ? vg_ : vq_);
    const unsigned v1 = wm == 2 ? vg_ : (wm < 2 ? (b * 7 + 4 * wm) * 4096 : b * 2 * 4096) + lane * 64;
#pragma unroll
    for (int i = 0; i < 4; ++i) { D.sa[i] = wm < 2 ? sx[i] : (wm == 2 ? sg[i] : sq[i]); D.sb[i] = wm < 2 ? sdn[i] : (wm == 2 ? su[i] : swo[i]); }
    D.stab = wm < 2 ? 128 : (wm == 2 ? 64 * 7168 : 64 * 6144); D.stc = wm == 2 ? 64 * 7168 : 4096;
    D.co[0] = 0; D.co[1] = 15; D.co[2] = wm == 2 ? 0 : (wm == 3 ? 6 : 4 + wm); D.co[3] = wm == 2 ? 15 : (wm == 3 ? 1 : 3 - wm);
    D.cc[0] = wm == 2 ? 128 : 43; D.cc[1] = wm == 2 ? 1 : 3;
    {
      u32x4 sb2 = D.sb; sb2[2] = wm == 2 ? sb2[2] : 0u;
#pragma unroll
      for (int t = 1; t < 3; ++t) { pfetch(D.sa, lb + K1_PF + wm * 512, v0, t * D.stab); pfetch(sb2, lb + K1_PF + wm * 512 + 256, v1, wm == 2 ? t * D.stc : 0u); }
    }
    k1_body<1>(acc, lb, wm, lane, sx, v0, v1, 0u, 0u, 0u, D);
    EPI_LANE();
    const int grow = b * 8 + 2 * wm + (lane >> 5);
    u32x4 gx[4], gw[4];
#pragma unroll
    for (int i = 0; i < 4; ++i) {
      gx[i] = *(const u32x4*)(x + grow * DM + i * 256 + (lane & 31) * 8);
      gw[i] = *(const u32x4*)(gatew + i * 256 + (lane & 31) * 8);
    }
#pragma unroll
    for (int mt = 0; mt < 4; ++mt) ST16(pack8(acc[mt][0], acc[mt][1]), WS_QF + (c * 128 + m16 + mt) * 1024);
    const int j0 = 7 * c + ((c & 1) ? 3 : 0);
    const unsigned hp = WS_AP + ((j0 >> 1) * 128 + m16) * 1024;
#pragma unroll
    for (int mt = 0; mt < 4; ++mt) {
      const f32x4 h0 = geglu4(acc[mt][2], acc[mt][6]), h1 = geglu4(acc[mt][3], acc[mt][7]);
      ST16(pack8(h0, h1), hp + mt * 1024);
      const f32x4 h2 = geglu4(acc[mt][4], acc[mt][8]), h3 = geglu4(acc[mt][5], acc[mt][9]);
      ST16(pack8(h2, h3), hp + 131072 + mt * 1024);
    }
    {
      float s = 0.f;
#pragma unroll
      for (int i = 0; i < 4; ++i)
#pragma unroll
        for (int e = 0; e < 4; ++e) {
          const unsigned pr = pk2(__uint_as_float(gx[i][e] << 16) * __uint_as_float(gw[i][e] << 16), __uint_as_float(gx[i][e] & 0xffff0000u) * __uint_as_float(gw[i][e] & 0xffff0000u));
          s += __uint_as_float(pr << 16) + __uint_as_float(pr & 0xffff0000u);
        }
#pragma unroll
      for (int o = 1; o < 32; o <<= 1) s += __shfl_xor(s, o);
      s = bfr(s);
      const float gv = bfr(1.0f / (1.0f + __expf(-s)));
      if ((lane & 31) == 0) ((float*)(ws + WS_G))[grow] = gv;
      if (wm == 0 && lane == 0) { ((unsigned*)(ws + WS_FLAGS))[(8 + b) * 16] = 0u; if (b < 8) ((unsigned*)(ws + WS_FLAGS))[b * 16] = 0u; }
    }
  }
  LGKM_BAR();
  {
    int tid = w * 64 + __builtin_amdgcn_mbcnt_hi(~0u, __builtin_amdgcn_mbcnt_lo(~0u, 0u)); asm volatile("" : "+v"(tid));
    const unsigned rd = lb + 65536 + tid * 16;
    const f32x4 s = *(LAS f32x4*)(rd) + *(LAS f32x4*)(rd + 8192) + *(LAS f32x4*)(rd + 16384) + *(LAS f32x4*)(rd + 24576);
    __builtin_amdgcn_raw_buffer_store_b64((u32x2){pk2(s[0], s[1]), pk2(s[2], s[3])}, rw, WS_KVP + ((c * 8 + w) * 4 + (rb >> 1)) * 1024 + (tid & 63) * 16 + (rb & 1) * 8, 0, 0);
    if (tid < 16) {
      const unsigned rz = lb + 65536 + 32768 + tid * 16;
      const f32x4 zs = *(LAS f32x4*)(rz) + *(LAS f32x4*)(rz + 256) + *(LAS f32x4*)(rz + 512) + *(LAS f32x4*)(rz + 768);
      *(f32x4*)(ws + WS_ZP + ((size_t)(c * 8 + rb) * 16 + tid) * 16) = zs;
    }
  }
}

constexpr unsigned K2_SLOT = 16384, K2_KV = 131072, K2_LDS = 131072 + 4608;
constexpr int NS = 18, NH = 14;
constexpr unsigned PSTR = 65536u;
constexpr int PAUX = 16;
constexpr int OUT_AUX = 16;
#define VM_BAR(N) do { asm volatile("" ::: "memory"); __builtin_amdgcn_s_waitcnt(((N) & 15) | 0xF70 | (((N) >> 4) << 14)); __builtin_amdgcn_s_barrier(); asm volatile("" ::: "memory"); } while (0)
#define BAR() do { asm volatile("" ::: "memory"); __builtin_amdgcn_s_barrier(); asm volatile("" ::: "memory"); } while (0)

DI void wait_ge(const unsigned* flag, unsigned need) {
  for (unsigned spin = 0; spin < (1u << 22); ++spin) {
    if (__hip_atomic_load((const GAS unsigned*)flag, __ATOMIC_RELAXED, __HIP_MEMORY_SCOPE_AGENT) >= need) break;
    __builtin_amdgcn_s_sleep(1);
  }
}
#define lda(d, srd, voff, soff) d = __builtin_bit_cast(bf16x8, __builtin_amdgcn_raw_buffer_load_b128(srd, voff, soff, 16))

__global__ __launch_bounds__(768, 3) void k2_kernel(const uint16_t* __restrict__ wdown, const uint16_t* __restrict__ wo, char* __restrict__ ws,
                                                    uint16_t* __restrict__ out) {
  extern __shared__ __attribute__((aligned(16))) char smem[];
  const unsigned lb = (unsigned)(size_t)(LAS char*)smem;
  const int lane = threadIdx.x & 63;
  const int w = __builtin_amdgcn_readfirstlane(threadIdx.x >> 6);
  const int g = lane >> 4;
  const int b = blockIdx.x, xj = b & 7, bi = b >> 3;
  const int kq = xj >> 1;
  const int mt = 4 * (xj & 1) + (bi >> 3), nt128 = bi & 7;
  const int tile = mt * 8 + nt128;
  const int oc = 4 * xj + (bi & 3), orb = bi >> 2;
  unsigned* const flags = (unsigned*)(ws + WS_FLAGS);
  const float* const gs = (const float*)(ws + WS_G);
  auto tix = [&](int s) -> int { return s < NH ? NH * kq + s : 56 - NH + 4 * kq + s; };

  if (w >= 8) {
    const int sw = w - 8;
    const u32x4 sd = mk_srd(wdown, DF * DM * 2), so_ = mk_srd(wo, DM * DM * 2);
    const unsigned vb0 = (16 * sw + g) * 2048 + nt128 * 256 + (((lane & 15) ^ (g << 1)) << 4), vb1 = (vb0 ^ 128) + 4 * 2048;
    auto stage = [&](int s) {
      if (s < NS) {
        u32x4 r;
#pragma unroll
        for (int j = 0; j < 4; ++j) r[j] = s >= NH ? so_[j] : sd[j];
        const unsigned so = (s >= NH ? 4 * kq + s - NH : NH * kq + s) * 131072, dst = lb + (s & 7) * K2_SLOT + sw * 4096;
#pragma unroll
        for (int i = 0; i < 4; ++i) dma16(r, dst + i * 1024, (i & 1) ? vb1 : vb0, so + (i >> 1) * 16384);
      }
    };
    stage(0); stage(1); stage(2); stage(3);
    VM_BAR(8);
    for (int k = 0; k < NS / 2 - 1; ++k) {
      stage(2 * k + 4); stage(2 * k + 5);
      if (k == 2 && threadIdx.x == 512)
        __hip_atomic_fetch_add((GAS unsigned*)(flags + orb * 16), 1u, __ATOMIC_RELAXED, __HIP_MEMORY_SCOPE_AGENT);
      if (k == 5 && sw == 3) {
        if (lane == 0) wait_ge(flags + mt * 16, 32u);
        asm volatile("s_waitcnt vmcnt(0)" ::: "memory");
      }
      if (2 * k + 4 < NS) VM_BAR(8); else VM_BAR(0);
    }
    BAR();
    if (sw == 0) {
      if (lane == 0) __hip_atomic_store((GAS unsigned*)(flags + (8 + tile * 4 + kq) * 16), 1u, __ATOMIC_RELAXED, __HIP_MEMORY_SCOPE_AGENT);
      if (lane < 4 && lane != kq) wait_ge(flags + (8 + tile * 4 + lane) * 16, 1u);
      asm volatile("s_waitcnt vmcnt(0)" ::: "memory");
      BAR();
    }
    return;
  }

  const int q = (lane & 15) >> 2, p = lane & 3;
  const auto sa = __builtin_amdgcn_make_buffer_rsrc((void*)(ws + WS_AP), 0, 144u * 131072u, 0x00020000);
  const unsigned va = lane * 16;
  const unsigned abase = (mt * 16 + 2 * w) * 1024;
  bf16x8 qf[2]; float go[2]; u32x4 kvl[4];
#pragma unroll
  for (int r = 0; r < 4; ++r) kvl[r] = *(const u32x4*)(ws + WS_KVP + (size_t)(oc * 8 + w) * 4096 + r * 1024 + va);
  const char* zp = ws + WS_ZP + ((size_t)oc * 8 * 16 + 2 * g * 16 + (lane & 15)) * 16;
  const f32x4 za_ = *(const f32x4*)zp, zb_ = *(const f32x4*)(zp + 256);
  asm volatile("" ::: "memory");
  const bf16x8 wp0 = *(const bf16x8*)(ws + WS_WPF + oc * 2048 + va), wp1 = *(const bf16x8*)(ws + WS_WPF + oc * 2048 + 1024 + va);
#pragma unroll
  for (int i = 0; i < 2; ++i) {
    qf[i] = *(const bf16x8*)(ws + WS_QF + ((size_t)oc * 128 + orb * 16 + 2 * w + i) * 1024 + va);
    go[i] = gs[(orb * 16 + 2 * w + i) * 16 + (lane & 15)];
  }
  asm volatile("" ::: "memory");
  bf16x8 ar[4][2];
  { const unsigned a0 = abase + tix(0) * 262144, a1 = abase + tix(1) * 262144;
    lda(ar[0][0], sa, va, a0); lda(ar[0][1], sa, va, a0 + 1024);
    lda(ar[1][0], sa, va, a0 + 131072); lda(ar[1][1], sa, va, a0 + 131072 + 1024);
    lda(ar[2][0], sa, va, a1); lda(ar[2][1], sa, va, a1 + 1024); }
  const float gsc0 = gs[mt * 256 + w * 32 + (lane & 15)], gsc1 = gs[mt * 256 + w * 32 + 16 + (lane & 15)];
  asm volatile("" ::: "memory");
  {
    f32x4 kvs = {0.f, 0.f, 0.f, 0.f};
#pragma unroll
    for (int r = 0; r < 4; ++r) kvs += unpk_lo(kvl[r]) + unpk_hi(kvl[r]);
    *(LAS u32x2*)(lb + K2_KV + ((w & 1) * 2 + (w >> 2)) * 1024 + va + ((w >> 1) & 1) * 8) = (u32x2){pk2(kvs[0], kvs[1]), pk2(kvs[2], kvs[3])};
    const f32x4 zs = za_ + zb_;
    float z0 = zs[0], z1 = zs[1], z2 = zs[2], z3 = zs[3]; XRED2(z0, z1, fadd_); XRED2(z2, z3, fadd_);
    if (w == 0) *(LAS u32x2*)(lb + K2_KV + 4096 + ((lane >> 3) & 1) * 64 + (lane & 3) * 16 + ((lane >> 2) & 1) * 8) = (u32x2){pk2(z0, z1), pk2(z2, z3)};
  }
  f32x4 acc[2][8];
#pragma unroll
  for (int m = 0; m < 2; ++m)
#pragma unroll
    for (int n = 0; n < 8; ++n) acc[m][n] = (f32x4){0.f, 0.f, 0.f, 0.f};
  const unsigned bb0 = lb + (4 * g + q) * 256 + (((4 * (g & 1) + q) << 1 | (p >> 1)) << 4) + (p & 1) * 8;
  unsigned ba[8];
#pragma unroll
  for (int n = 0; n < 8; ++n) ba[n] = bb0 ^ (n << 5);
  unsigned sp2 = 0;
  bf16x8 bw[8];
  auto ldB = [&](int par, int u) -> bf16x8 { const unsigned a = ba[u & 7] + par * K2_SLOT + (u >> 3) * 8192; return trrd2(a, a + 4096); };
  __builtin_amdgcn_s_waitcnt(0xC07F);
  BAR();
  {
    bf16x8 ka[2][2], za[2];
#pragma unroll
    for (int ks = 0; ks < 2; ++ks) {
      ka[0][ks] = ldsrd(lb + K2_KV + (0 + ks) * 1024 + va);
      ka[1][ks] = ldsrd(lb + K2_KV + (2 + ks) * 1024 + va);
      za[ks] = ldsrd(lb + K2_KV + 4096 + ks * 64 + g * 16);
    }
#pragma unroll
    for (int i = 0; i < 2; ++i) {
      const f32x4 z4 = {0.f, 0.f, 0.f, 0.f};
      f32x4 pp0, pp1, pn0, pn1;
      featmap(qf[i], wp0, wp1, pp0, pp1, pn0, pn1);
      const bf16x8 pf0 = pack8(pp0, pp1), pf1 = pack8(pn0, pn1);
      f32x4 n0 = mfma16(ka[0][1], pf1, mfma16(ka[0][0], pf0, z4));
      f32x4 n1 = mfma16(ka[1][1], pf1, mfma16(ka[1][0], pf0, z4));
      f32x4 dn = mfma16(za[1], pf1, mfma16(za[0], pf0, z4));
      const float sc = go[i] * __builtin_amdgcn_rcpf(dn[0] + 1e-6f);
      n0 *= sc; n1 *= sc;
      __builtin_amdgcn_raw_buffer_store_b128(__builtin_bit_cast(u32x4, pack8(n0, n1)), sa, va + ((112 + oc) * 128 + orb * 16 + 2 * w + i) * 1024, 0, 16  );
    }
    __builtin_amdgcn_s_waitcnt(0xC07F);
    asm volatile("" ::: "memory");
  }
  SB(); MFMA_FENCE();
#pragma unroll
  for (int u = 0; u < 6; ++u) bw[u] = ldB(0, u);
  const auto rp = __builtin_amdgcn_make_buffer_rsrc((void*)(ws + WS_PART), 0, 256u * PSTR, 0x00020000);
  const unsigned ex = lb + 32768;
  const bool own = (w >> 1) == kq;
  auto emit = [&](int n) {
    if (own) {
#pragma unroll
      for (int m = 0; m < 2; ++m) { *(LAS f32x4*)(ex + (((w & 1) * 2 + m) * 8 + 2 * n) * 1024 + va) = acc[m][2 * n]; *(LAS f32x4*)(ex + (((w & 1) * 2 + m) * 8 + 2 * n + 1) * 1024 + va) = acc[m][2 * n + 1]; }
    } else {
#pragma unroll
      for (int m = 0; m < 2; ++m)
        __builtin_amdgcn_raw_buffer_store_b128(__builtin_bit_cast(u32x4, pack8(acc[m][2 * n], acc[m][2 * n + 1])), rp, va + (tile * 4 + kq) * PSTR + w * 8192 + m * 4096 + n * 1024, 0, PAUX);
    }
  };
  auto ldF = [&](int v) -> bf16x8 { const int blk = (v >> 2) & 3; return ldB(blk >> 1, (blk & 1) * 8 + 4 * (v >> 4) + (v & 3)); };
  auto step = [&](int s, auto par_, auto fin_) {
    constexpr int par = decltype(par_)::value;
    constexpr bool ia0 = true, ia1 = true, nxt = true, fin = decltype(fin_)::value;
    const unsigned an1 = abase + tix(s + 1) * 262144 + 131072, an2 = abase + tix(s + 2) * 262144;
#pragma unroll
    for (int u = 0; u < 16; ++u) {
      if (u == 10 && nxt && par == 1) {
        VM_BAR(8);
        const unsigned nx = (sp2 + 32768) & 98304, dx = sp2 ^ nx; sp2 = nx;
#pragma unroll
        for (int n = 0; n < 8; ++n) { ba[n] ^= dx; asm volatile("" : "+v"(ba[n])); }
      }
      if (u < 10) bw[(u + 6) & 7] = ldB(par, u + 6);
      else bw[(u + 6) & 7] = fin ? ldF(u - 10) : ldB(par ^ 1, u - 10);
      if (ia0 && u < 2) lda(ar[(2 * par + 3) & 3][u], sa, va, an1 + u * 1024);
      if (ia1 && (u == 8 || u == 9)) lda(ar[2 * par][u - 8], sa, va, an2 + (u - 8) * 1024);
      if ((u & 1) == 0 && (u < 10 || nxt)) __builtin_amdgcn_s_waitcnt(0xCA7F);
#pragma unroll
      for (int m = 0; m < 2; ++m) mfma_ip(acc[m][u & 7], bw[u & 7], ar[2 * par + (u >> 3)][m]);
      SB();
    }
  };
#pragma unroll 1
  for (int s = 0; s < NH; s += 2) { step(s, I0{}, I0{}); step(s + 1, I1{}, I0{}); }
  {
    MFMA_FENCE(); SB();
    const float s0 = bfr(1.0f - gsc0), s1 = bfr(1.0f - gsc1);
#pragma unroll
    for (int n = 0; n < 8; ++n) { acc[0][n] *= s0; acc[1][n] *= s1; }
    SB(); MFMA_FENCE();
  }
  step(NH, I0{}, I0{}); step(NH + 1, I1{}, I1{});
  {
    const unsigned an1 = abase + tix(NS - 1) * 262144 + 131072;
#pragma unroll
    for (int v = 0; v < 32; ++v) {
      if (v + 6 < 32) bw[(v + 6) & 7] = ldF(v + 6);
      if (v < 2) lda(ar[3][v], sa, va, an1 + v * 1024);
      if ((v & 1) == 0 && v + 6 < 32) __builtin_amdgcn_s_waitcnt(0xCA7F);
#pragma unroll
      for (int m = 0; m < 2; ++m) mfma_ip(acc[m][4 * (v >> 4) + (v & 3)], bw[v & 7], ar[(v >> 2) & 3][m]);
      SB();
      if (v == 17) { emit(0); emit(1); SB(); }
    }
  }
  MFMA_FENCE(); SB();
  emit(2); emit(3);
  WAIT_BAR(0);
  f32x4 fin[4];
#pragma unroll
  for (int n = 0; n < 4; ++n) fin[n] = *(LAS f32x4*)(ex + ((w >> 1) * 8 + 4 * (w & 1) + n) * 1024 + va);
  BAR();
  {
    u32x4 part[3][2];
#pragma unroll
    for (int j = 0; j < 3; ++j) {
      const int src = (kq + 1 + j) & 3;
      const unsigned po = (tile * 4 + src) * PSTR + (2 * kq + (w >> 2)) * 8192 + ((w >> 1) & 1) * 4096 + 2 * (w & 1) * 1024;
      part[j][0] = __builtin_amdgcn_raw_buffer_load_b128(rp, va, po, 16);
      part[j][1] = __builtin_amdgcn_raw_buffer_load_b128(rp, va, po + 1024, 16);
    }
    const auto ro = __builtin_amdgcn_make_buffer_rsrc((void*)out, 0, SEQ * DM * 2, 0x00020000);
    u32x4 ch[2];
#pragma unroll
    for (int n = 0; n < 2; ++n) {
      const f32x4 v0 = fin[2 * n] + (unpk_lo(part[0][n]) + unpk_lo(part[1][n]) + unpk_lo(part[2][n]));
      const f32x4 v1 = fin[2 * n + 1] + (unpk_hi(part[0][n]) + unpk_hi(part[1][n]) + unpk_hi(part[2][n]));
      const auto r0 = __builtin_amdgcn_permlane16_swap(pk2(v0[0], v0[1]), pk2(v1[0], v1[1]), false, false);
      const auto r1 = __builtin_amdgcn_permlane16_swap(pk2(v0[2], v0[3]), pk2(v1[2], v1[3]), false, false);
      ch[n] = (u32x4){r0[0], r1[0], r0[1], r1[1]};
    }
    const bool lo = (lane & 8) == 0;
    u32x4 sa_, sb_;
#pragma unroll
    for (int e = 0; e < 4; ++e) {
      const unsigned t = (unsigned)__builtin_amdgcn_update_dpp(0, (int)(lo ? ch[1][e] : ch[0][e]), 0x128  , 0xf, 0xf, false);
      sa_[e] = lo ? ch[0][e] : t; sb_[e] = lo ? t : ch[1][e];
    }
    const unsigned oo = ((mt * 256 + kq * 64 + (w >> 1) * 16 + (lane & 7)) * DM + nt128 * 128 + (w & 1) * 64 + ((lane >> 3) & 1) * 32 + (g & 1) * 16 + (g >> 1) * 8) * 2;
    __builtin_amdgcn_raw_buffer_store_b128(sa_, ro, oo, 0, OUT_AUX);
    __builtin_amdgcn_raw_buffer_store_b128(sb_, ro, oo + 8 * DM * 2, 0, OUT_AUX);
  }
}

extern "C" void kernel_launch(void* const* d_in, const int* in_sizes, int n_in, void* d_out, int out_size, void* d_ws, size_t ws_size,
                              hipStream_t stream) {
  const uint16_t* x = (const uint16_t*)d_in[0];
  const uint16_t* wqkv = (const uint16_t*)d_in[1];
  const uint16_t* wphi = (const uint16_t*)d_in[2];
  const uint16_t* wo = (const uint16_t*)d_in[3];
  const uint16_t* wg = (const uint16_t*)d_in[5];
  const uint16_t* wu = (const uint16_t*)d_in[6];
  const uint16_t* wd = (const uint16_t*)d_in[7];
  const uint16_t* gw = (const uint16_t*)d_in[8];
  (void)hipFuncSetAttribute((const void*)k1_kernel, hipFuncAttributeMaxDynamicSharedMemorySize, K1_LDS);
  (void)hipFuncSetAttribute((const void*)k2_kernel, hipFuncAttributeMaxDynamicSharedMemorySize, K2_LDS);
  k1_kernel<<<256, 512, K1_LDS, stream>>>(x, wqkv, wphi, wg, wu, gw, wd, wo, (char*)d_ws);
  k2_kernel<<<256, 768, K2_LDS, stream>>>(wd, wo, (char*)d_ws, (uint16_t*)d_out);
}
```
